```python
import torch

def _softmax(x, axis=-1):
    return torch.softmax(x, dim=axis)
def _gelu(x):
    return torch.nn.functional.gelu(x, approximate="tanh")
def _silu(x):
    return torch.nn.functional.silu(x)
def _sigmoid(x):
    return torch.sigmoid(x)
def _layernorm(x, axis=-1, eps=1e-5):
    m = x.mean(dim=axis, keepdim=True)
    v = x.var(dim=axis, keepdim=True, unbiased=False)
    return (x - m) / torch.sqrt(v + eps)
def _scatter_add(tgt, idx, src, axis=0):
    return tgt.clone().index_add_(axis, idx, src)
def _segment_sum(src, seg, n):
    out = torch.zeros((n,) + src.shape[1:], dtype=src.dtype)
    return out.index_add_(0, seg, src)
def _pad(x, pw):
    flat = tuple(v for pair in reversed(pw) for v in pair)
    return torch.nn.functional.pad(x, flat)

kernel_name = 'interleave_layers__sliding_local_dilated__mixer_s_mlp__prompt'

def setup_inputs():

    _w = (lambda a: (lambda t: t.to(torch.bfloat16) if t.is_floating_point() else t)(torch.as_tensor(a)))
    def _A_setup():
        B, S, D = 1, 2048, 448
        return {
            "x": _w((lambda *s: torch.randn(*s, dtype=torch.bfloat16) * 0.05)(B, S, D)),
            "wqkv": _w((lambda *s: torch.randn(*s, dtype=torch.bfloat16) * 0.1)(D, 3 * D)),
            "wo": _w((lambda *s: torch.randn(*s, dtype=torch.bfloat16) * 0.1)(D, D)),
        }
    _A_in = _A_setup()

    def _B_setup():
        B, S, D, F = 1, 2048, 448, 1792
        return {
            "x":  _w((lambda *s: torch.randn(*s, dtype=torch.bfloat16) * 0.05)(B, S, D)),
            "w1": _w((lambda *s: torch.randn(*s, dtype=torch.bfloat16) * 3.2)(D, D) * (D ** -0.5)),
            "w2": _w((lambda *s: torch.randn(*s, dtype=torch.bfloat16) * 3.2)(D, D) * (D ** -0.5)),
            "w3": _w((lambda *s: torch.randn(*s, dtype=torch.bfloat16) * 3.2)(D, F) * (D ** -0.5)),
            "w4": _w((lambda *s: torch.randn(*s, dtype=torch.bfloat16) * 3.2)(F, D) * (F ** -0.5)),
        }
    _B_in = _B_setup()

    return {
        "A_x": _A_in["x"],
        "A_wqkv": _A_in["wqkv"],
        "A_wo": _A_in["wo"],
        "B_x": _B_in["x"],
        "B_w1": _B_in["w1"],
        "B_w2": _B_in["w2"],
        "B_w3": _B_in["w3"],
        "B_w4": _B_in["w4"],
    }

def reference(A_x, A_wqkv, A_wo, B_x, B_w1, B_w2, B_w3, B_w4):
    def _A_ref(x, wqkv, wo):
        B, S, D = x.shape
        H = 16; Dh = D // H
        W, dil = 8, 2
        qkv = (lambda x, *d: x.permute(*d))((x @ wqkv).reshape(B, S, 3, H, Dh), 2, 0, 3, 1, 4)
        q, k, v = qkv[0], qkv[1], qkv[2]
        scale = Dh ** -0.5
        pos = torch.arange(S)
        off = torch.arange(W) * dil
        idx = pos[:, None] - off[None, :]
        mask = idx >= 0
        idx_c = torch.where(mask, idx, 0).reshape(-1)
        kg = k[:, :, idx_c, :].reshape(B, H, S, W, Dh)
        vg = v[:, :, idx_c, :].reshape(B, H, S, W, Dh)
        logits = torch.einsum("bhsd,bhswd->bhsw", q, kg) * scale
        logits = torch.where(mask[None, None, :, :], logits, -1e9)
        attn = _softmax(logits, axis=-1)
        o = torch.einsum("bhsw,bhswd->bhsd", attn, vg)
        return (lambda x, *d: x.permute(*d))(o, 0, 2, 1, 3).reshape(B, S, D) @ wo

    def _B_ref(x, w1, w2, w3, w4):
        B, S, D = x.shape
        h = _gelu(_layernorm(x, axis=-1) @ w1)
        c = D // 4
        g0 = _pad(h[:, :, 0*c:1*c], ((0, 0), (1, 0), (0, 0)))[:, :S, :]
        g1 = _pad(h[:, :, 1*c:2*c], ((0, 0), (0, 1), (0, 0)))[:, 1:, :]
        g2 = _pad(h[:, :, 2*c:3*c], ((0, 0), (2, 0), (0, 0)))[:, :S, :]
        g3 = _pad(h[:, :, 3*c:   ], ((0, 0), (0, 2), (0, 0)))[:, 2:, :]
        hs = torch.concatenate([g0, g1, g2, g3], axis=-1) @ w2
        x1 = x + hs
        h2 = _gelu(_layernorm(x1, axis=-1) @ w3) @ w4
        return x1 + h2

    x = A_x
    x = x + _A_ref(_layernorm(x, axis=-1), A_wqkv, A_wo)
    x = x + _B_ref(_layernorm(x, axis=-1), B_w1, B_w2, B_w3, B_w4)
    x = x + _A_ref(_layernorm(x, axis=-1), A_wqkv, A_wo)
    x = x + _B_ref(_layernorm(x, axis=-1), B_w1, B_w2, B_w3, B_w4)
    return x
```

```cpp
#include <hip/hip_runtime.h>
#include <hip/hip_bf16.h>
#include <stdint.h>

typedef __hip_bfloat16 bf16;
#define S_ 2048
#define D_ 448
#define H_ 16
#define DH_ 28
#define F_ 1792

__device__ __forceinline__ float b2f(bf16 v) { return __bfloat162float(v); }
__device__ __forceinline__ bf16 f2b(float v) { return __float2bfloat16(v); }
__device__ __forceinline__ float rb(float v) { return __bfloat162float(__float2bfloat16(v)); }

__global__ void ln_kernel(const bf16* __restrict__ x, bf16* __restrict__ y) {
    int r = blockIdx.x;
    __shared__ float red[256];
    int t = threadIdx.x;
    float s = 0.f;
    for (int i = t; i < D_; i += 256) s += b2f(x[r * D_ + i]);
    red[t] = s; __syncthreads();
    for (int o = 128; o > 0; o >>= 1) { if (t < o) red[t] += red[t + o]; __syncthreads(); }
    float mean = red[0] / D_; __syncthreads();
    float q = 0.f;
    for (int i = t; i < D_; i += 256) { float d = b2f(x[r * D_ + i]) - mean; q += d * d; }
    red[t] = q; __syncthreads();
    for (int o = 128; o > 0; o >>= 1) { if (t < o) red[t] += red[t + o]; __syncthreads(); }
    float var = red[0] / D_;
    float mb = rb(mean), vb = rb(var);
    float den = rb(sqrtf(rb(vb + 1e-5f)));
    for (int i = t; i < D_; i += 256) {
        float d = rb(b2f(x[r * D_ + i]) - mb);
        y[r * D_ + i] = f2b(d / den);
    }
}

__global__ void mm_kernel(const bf16* __restrict__ A, const bf16* __restrict__ B, bf16* __restrict__ C, int M, int N, int K, int epi) {
    int n = blockIdx.x * blockDim.x + threadIdx.x;
    int m = blockIdx.y;
    if (n >= N) return;
    float acc = 0.f;
    for (int k = 0; k < K; ++k) acc += b2f(A[m * K + k]) * b2f(B[k * N + n]);
    float v = rb(acc);
    if (epi == 1) {
        float u = v;
        float inner = 0.7978845608028654f * (u + 0.044715f * u * u * u);
        v = 0.5f * u * (1.f + tanhf(inner));
    }
    C[m * N + n] = f2b(v);
}

__global__ void attn_kernel(const bf16* __restrict__ qkv, bf16* __restrict__ o) {
    int idx = blockIdx.x * blockDim.x + threadIdx.x;
    if (idx >= S_ * H_) return;
    int s = idx / H_, h = idx % H_;
    const bf16* q = qkv + (size_t)s * (3 * D_) + h * DH_;
    float lg[8];
    const float scale = (float)0.18898223650461361;
    for (int w = 0; w < 8; ++w) {
        int p = s - 2 * w;
        if (p < 0) { lg[w] = rb(-1e9f); continue; }
        const bf16* k = qkv + (size_t)p * (3 * D_) + D_ + h * DH_;
        float acc = 0.f;
        for (int d = 0; d < DH_; ++d) acc += b2f(q[d]) * b2f(k[d]);
        lg[w] = rb(rb(acc) * scale);
    }
    float mx = lg[0];
    for (int w = 1; w < 8; ++w) mx = fmaxf(mx, lg[w]);
    float e[8], sum = 0.f;
    for (int w = 0; w < 8; ++w) { e[w] = expf(lg[w] - mx); sum += e[w]; }
    for (int w = 0; w < 8; ++w) e[w] = rb(e[w] / sum);
    for (int d = 0; d < DH_; ++d) {
        float acc = 0.f;
        for (int w = 0; w < 8; ++w) {
            int p = s - 2 * w; if (p < 0) p = 0;
            const bf16* v = qkv + (size_t)p * (3 * D_) + 2 * D_ + h * DH_;
            acc += e[w] * b2f(v[d]);
        }
        o[(size_t)s * D_ + h * DH_ + d] = f2b(acc);
    }
}

__global__ void add_kernel(const bf16* __restrict__ a, const bf16* __restrict__ b, bf16* __restrict__ c, int n) {
    int i = blockIdx.x * blockDim.x + threadIdx.x;
    if (i < n) c[i] = f2b(b2f(a[i]) + b2f(b[i]));
}

__global__ void shift_kernel(const bf16* __restrict__ h, bf16* __restrict__ g) {
    int i = blockIdx.x * blockDim.x + threadIdx.x;
    if (i >= S_ * D_) return;
    int s = i / D_, c = i % D_;
    int grp = c / (D_ / 4);
    int src = (grp == 0) ? s - 1 : (grp == 1) ? s + 1 : (grp == 2) ? s - 2 : s + 2;
    g[i] = (src >= 0 && src < S_) ? h[src * D_ + c] : f2b(0.f);
}

extern "C" void kernel_launch(void* const* d_in, const int* in_sizes, int n_in,
                              void* d_out, int out_size, void* d_ws, size_t ws_size,
                              hipStream_t stream) {
    const bf16* x0 = (const bf16*)d_in[0];
    const bf16* wqkv = (const bf16*)d_in[1];
    const bf16* wo = (const bf16*)d_in[2];
    const bf16* w1 = (const bf16*)d_in[4];
    const bf16* w2 = (const bf16*)d_in[5];
    const bf16* w3 = (const bf16*)d_in[6];
    const bf16* w4 = (const bf16*)d_in[7];
    bf16* out = (bf16*)d_out;
    char* ws = (char*)d_ws;
    size_t sd = (size_t)S_ * D_ * 2;
    bf16* xa = (bf16*)(ws + 0 * sd);
    bf16* xb = (bf16*)(ws + 1 * sd);
    bf16* ln = (bf16*)(ws + 2 * sd);
    bf16* ln2 = (bf16*)(ws + 3 * sd);
    bf16* t0 = (bf16*)(ws + 4 * sd);
    bf16* t1 = (bf16*)(ws + 5 * sd);
    bf16* t2 = (bf16*)(ws + 6 * sd);
    bf16* big = (bf16*)(ws + 8 * sd);
    const bf16* x = x0;
    for (int it = 0; it < 2; ++it) {
        ln_kernel<<<S_, 256, 0, stream>>>(x, ln);
        mm_kernel<<<dim3((3 * D_ + 255) / 256, S_), 256, 0, stream>>>(ln, wqkv, big, S_, 3 * D_, D_, 0);
        attn_kernel<<<(S_ * H_ + 255) / 256, 256, 0, stream>>>(big, t0);
        mm_kernel<<<dim3((D_ + 255) / 256, S_), 256, 0, stream>>>(t0, wo, t1, S_, D_, D_, 0);
        add_kernel<<<(S_ * D_ + 255) / 256, 256, 0, stream>>>(x, t1, xa, S_ * D_);
        ln_kernel<<<S_, 256, 0, stream>>>(xa, ln);
        ln_kernel<<<S_, 256, 0, stream>>>(ln, ln2);
        mm_kernel<<<dim3((D_ + 255) / 256, S_), 256, 0, stream>>>(ln2, w1, t0, S_, D_, D_, 1);
        shift_kernel<<<(S_ * D_ + 255) / 256, 256, 0, stream>>>(t0, t1);
        mm_kernel<<<dim3((D_ + 255) / 256, S_), 256, 0, stream>>>(t1, w2, t0, S_, D_, D_, 0);
        add_kernel<<<(S_ * D_ + 255) / 256, 256, 0, stream>>>(ln, t0, t2, S_ * D_);
        ln_kernel<<<S_, 256, 0, stream>>>(t2, ln2);
        mm_kernel<<<dim3((F_ + 255) / 256, S_), 256, 0, stream>>>(ln2, w3, big, S_, F_, D_, 1);
        mm_kernel<<<dim3((D_ + 255) / 256, S_), 256, 0, stream>>>(big, w4, t0, S_, D_, F_, 0);
        add_kernel<<<(S_ * D_ + 255) / 256, 256, 0, stream>>>(t2, t0, t1, S_ * D_);
        bf16* dst = (it == 1) ? out : xb;
        add_kernel<<<(S_ * D_ + 255) / 256, 256, 0, stream>>>(xa, t1, dst, S_ * D_);
        x = xb;
    }
}
```

```cpp
#include <hip/hip_runtime.h>
#include <hip/hip_bf16.h>
#include <stdint.h>
#include <type_traits>

#define SC1AUX 16
#define SC1DMA 1
namespace {

typedef unsigned short u16;
typedef __attribute__((ext_vector_type(8))) short bf16x8;
typedef __attribute__((ext_vector_type(4))) short s16x4;
typedef __attribute__((ext_vector_type(4))) float f32x4;
typedef __attribute__((ext_vector_type(4))) unsigned u32x4;
typedef __attribute__((ext_vector_type(2))) unsigned u32x2;
typedef __attribute__((ext_vector_type(2))) float f32x2;
typedef __attribute__((ext_vector_type(2))) __bf16 bf2_t;
typedef __attribute__((address_space(3))) s16x4 lds_s16x4;

constexpr int S_ = 2048, D_ = 448, F_ = 1792;
constexpr int NT_ = 512;
constexpr float LN_EPS = 1e-5f;

constexpr size_t WS_CNT = 0;
constexpr size_t CNT_BYTES = 16 * 32 * 256;
constexpr size_t SD2 = (size_t)S_ * D_ * 2;
constexpr size_t WS_L0 = CNT_BYTES;
constexpr size_t LAYER_BYTES = 4 * SD2 + (size_t)S_ * 8 + (size_t)S_ * F_ * 2;
constexpr size_t OFF_O = 0, OFF_X2 = SD2, OFF_H = 2 * SD2, OFF_X1 = 3 * SD2, OFF_ST = 4 * SD2, OFF_H2 = 4 * SD2 + (size_t)S_ * 8;
constexpr size_t WS_X3 = WS_L0 + 2 * LAYER_BYTES;
constexpr size_t WS_CS = WS_X3 + SD2;
constexpr size_t CS_QKV = 0, CS_W1 = 1344, CS_W3 = 1344 + 448;
constexpr int NCS = 1344 + 448 + 1792;

struct Params {
    const u16* x; const u16* wqkv; const u16* wo; const u16* w1; const u16* w2; const u16* w3; const u16* w4;
    u16* out; char* ws;
};

__device__ __forceinline__ float bf2f(u16 h) { return __builtin_bit_cast(float, (unsigned)h << 16); }
__device__ __forceinline__ u16 f2bf(float f) { return __builtin_bit_cast(u16, __float2bfloat16(f)); }
__device__ __forceinline__ float rbf(float f) { return bf2f(f2bf(f)); }
__device__ __forceinline__ unsigned pack2(float lo, float hi) { return (unsigned)f2bf(lo) | ((unsigned)f2bf(hi) << 16); }
__device__ __forceinline__ float lo_f(unsigned u) { return __builtin_bit_cast(float, u << 16); }
__device__ __forceinline__ float hi_f(unsigned u) { return __builtin_bit_cast(float, u & 0xffff0000u); }
__device__ __forceinline__ float dot2(unsigned a, unsigned b, float c) {
    return __builtin_amdgcn_fdot2_f32_bf16(__builtin_bit_cast(bf2_t, a), __builtin_bit_cast(bf2_t, b), c, false);
}
__device__ __forceinline__ float gelu_tanh(float u) {
    float t = 0.7978845608028654f * (u + 0.044715f * u * u * u);
    float e = __builtin_amdgcn_exp2f(-2.885390081777927f * t);
    return u * __builtin_amdgcn_rcpf(1.f + e);
}

template <int N> __device__ __forceinline__ void wait_vm() { asm volatile("s_waitcnt vmcnt(%0)" :: "n"(N) : "memory"); }
__device__ __forceinline__ void wg_barrier() {
    asm volatile("s_waitcnt lgkmcnt(0)" ::: "memory");
    __builtin_amdgcn_s_barrier();
    asm volatile("" ::: "memory");
}
template <int I, int N, class F> __device__ __forceinline__ void sfor(F&& f) {
    if constexpr (I < N) { f(std::integral_constant<int, I>{}); sfor<I + 1, N>(f); }
}

typedef __amdgpu_buffer_rsrc_t rsrc_t;
__device__ __forceinline__ rsrc_t mk_rsrc(const void* p, unsigned bytes) {
    return __builtin_amdgcn_make_buffer_rsrc((void*)p, (short)0, bytes, 0x00020000);
}
template <int SC1>
__device__ __forceinline__ void dma16(rsrc_t srd, unsigned voff, unsigned soff, unsigned lds_base) {
    if constexpr (SC1)
        asm volatile("s_mov_b32 m0, %0\n\ts_nop 0\n\tbuffer_load_dwordx4 %1, %2, %3 offen sc1 lds" :: "s"(lds_base), "v"(voff), "s"(srd), "s"(soff) : "memory");
    else
        asm volatile("s_mov_b32 m0, %0\n\ts_nop 0\n\tbuffer_load_dwordx4 %1, %2, %3 offen lds" :: "s"(lds_base), "v"(voff), "s"(srd), "s"(soff) : "memory");
}
__device__ __forceinline__ bf16x8 ld_tr(const char* p0, const char* p1) {
    s16x4 lo = __builtin_amdgcn_ds_read_tr16_b64_v4i16((lds_s16x4*)(p0));
    s16x4 hi = __builtin_amdgcn_ds_read_tr16_b64_v4i16((lds_s16x4*)(p1));
    bf16x8 r; r[0] = lo[0]; r[1] = lo[1]; r[2] = lo[2]; r[3] = lo[3]; r[4] = hi[0]; r[5] = hi[1]; r[6] = hi[2]; r[7] = hi[3];
    return r;
}

template <int NCHUNK, int D, int APW, int BPW> constexpr int ring_nout(int kc) {
    int cnt = 0;
    if (kc < D) {
        cnt += (D - 1 - kc) * APW;
        for (int i = 1; i < kc; ++i) if (i - 1 + D < NCHUNK) cnt += APW + BPW;
    } else {
        for (int i = kc - D + 2; i < kc; ++i) if (i - 1 + D < NCHUNK) cnt += APW + BPW;
    }
    return cnt;
}
template <int NCHUNK, int D, int APW, int BPW, class IA, class IB, class CP>
__device__ __forceinline__ void ring_loop(IA&& issueA, IB&& issueB, CP&& compute) {
    sfor<0, NCHUNK>([&](auto KC) {
        constexpr int kc = decltype(KC)::value;
        wait_vm<ring_nout<NCHUNK, D, APW, BPW>(kc)>();
        __builtin_amdgcn_s_barrier();
        asm volatile("" ::: "memory");
        if constexpr (kc >= 1 && kc - 1 + D < NCHUNK) {
            issueA(std::integral_constant<int, kc - 1 + D>{});
            issueB(std::integral_constant<int, kc - 1 + D>{});
        }
        compute(KC);
    });
}

__device__ __forceinline__ void stat_acc(const bf16x8& f, float& s, float& ss) {
    u32x4 u = __builtin_bit_cast(u32x4, f);
#pragma unroll
    for (int i = 0; i < 4; ++i) { s = dot2(u[i], 0x3F803F80u, s); ss = dot2(u[i], u[i], ss); }
}
__device__ __forceinline__ void sum_acc(const bf16x8& f, float& s) {
    u32x4 u = __builtin_bit_cast(u32x4, f);
#pragma unroll
    for (int i = 0; i < 4; ++i) s = dot2(u[i], 0x3F803F80u, s);
}
__device__ __forceinline__ float grp_reduce(float v) {
    v += __shfl_xor(v, 16);
    v += __shfl_xor(v, 32);
    return v;
}

enum { AM_PLAIN = 0, AM_SHIFT = 1 };
enum { EP_S2 = 2, EP_S3 = 3, EP_S4 = 4, EP_S5 = 5, EP_S6 = 6 };

template <int EPI_> struct GCfg {
    static constexpr int EPI = EPI_;
    static constexpr int BM = 64;
    static constexpr int BN = (EPI_ == EP_S5) ? 256 : 64;
    static constexpr int KTOT = (EPI_ == EP_S6) ? F_ : D_;
    static constexpr int NCHUNK = KTOT / 64;
    static constexpr int D = (EPI_ == EP_S5) ? 3 : (EPI_ == EP_S6) ? 8 : 7;
    static constexpr int NTN = ((EPI_ == EP_S5) ? F_ : D_) / BN;
    static constexpr int LDW = (EPI_ == EP_S5) ? F_ : D_;
    static constexpr int MT = (EPI_ == EP_S5) ? 2 : 1;
    static constexpr int NT = (EPI_ == EP_S5) ? 4 : 2;
    static constexpr int WM = BM / 16 / MT;
    static constexpr int WN = 8 / WM;
    static constexpr int APW = BM / 64;
    static constexpr int BPW = BN / 64;
    static constexpr int A_BYTES = BM * 128, B_BYTES = 64 * BN * 2, SLOT = A_BYTES + B_BYTES;
    static constexpr bool LN = (EPI_ == EP_S3 || EPI_ == EP_S5);
    static constexpr int AMODE = (EPI_ == EP_S4) ? AM_SHIFT : AM_PLAIN;
    static constexpr int NTILES = (S_ / BM) * NTN;
};

template <class C>
__device__ __forceinline__ const u16* stage_w(const Params& P) {
    if constexpr (C::EPI == EP_S2) return P.wo;
    else if constexpr (C::EPI == EP_S3) return P.w1;
    else if constexpr (C::EPI == EP_S4) return P.w2;
    else if constexpr (C::EPI == EP_S5) return P.w3;
    else return P.w4;
}

template <class C>
__device__ __forceinline__ void g_issueB(rsrc_t srdW, const unsigned (&bvoff)[C::BPW], int kc, unsigned lds0, int w) {
#pragma unroll
    for (int i = 0; i < C::BPW; ++i)
        dma16<0>(srdW, bvoff[i], (unsigned)(kc * 64 * C::LDW * 2),
                 lds0 + (unsigned)((kc % C::D) * C::SLOT + C::A_BYTES + (w * C::BPW + i) * 1024));
}
template <class C>
__device__ __forceinline__ void g_bvoff(unsigned (&bvoff)[C::BPW], int n0, int w, int l) {
    constexpr int U = C::BN / 8;
#pragma unroll
    for (int i = 0; i < C::BPW; ++i) {
        int u = 64 * (w * C::BPW + i) + l;
        int k = u / U, pu = u % U;
        int h = ((k >> 1) & 1) | (((k >> 3) & 1) << 1);
        int lu32 = (pu >> 1) ^ h;
        bvoff[i] = (unsigned)((k * C::LDW + n0 + lu32 * 16 + (pu & 1) * 8) * 2);
    }
}

template <class C>
__device__ __forceinline__ void gemm_prefetch(const Params& P, int tile, unsigned lds0) {
    const int w = __builtin_amdgcn_readfirstlane(threadIdx.x >> 6), l = threadIdx.x & 63;
    const int n0 = (tile % C::NTN) * C::BN;
    rsrc_t srdW = mk_rsrc(stage_w<C>(P), (unsigned)(C::KTOT * C::LDW * 2));
    unsigned bvoff[C::BPW];
    g_bvoff<C>(bvoff, n0, w, l);
#pragma unroll
    for (int j = 0; j < C::D; ++j) g_issueB<C>(srdW, bvoff, j, lds0, w);
}

template <class C>
__device__ __forceinline__ void gemm_main(const Params& P, int layer, int tile, char* smem) {
    const unsigned lds0 = (unsigned)(size_t)smem;
    const int tid = threadIdx.x, w = __builtin_amdgcn_readfirstlane(tid >> 6), l = tid & 63;
    const int tm = tile / C::NTN, tn = tile % C::NTN;
    const int r0 = tm * C::BM, n0 = tn * C::BN;
    char* lay = P.ws + WS_L0 + (size_t)layer * LAYER_BYTES;
    const u16* xres = (layer == 0) ? P.x : (const u16*)(P.ws + WS_X3);
    const u16* Ain; u16* Cout; unsigned ldA, ldC;
    if constexpr (C::EPI == EP_S2) { Ain = (const u16*)(lay + OFF_O); Cout = (u16*)(lay + OFF_X2); }
    else if constexpr (C::EPI == EP_S3) { Ain = (const u16*)(lay + OFF_X2); Cout = (u16*)(lay + OFF_H); }
    else if constexpr (C::EPI == EP_S4) { Ain = (const u16*)(lay + OFF_H); Cout = (u16*)(lay + OFF_X1); }
    else if constexpr (C::EPI == EP_S5) { Ain = (const u16*)(lay + OFF_X1); Cout = (u16*)(lay + OFF_H2); }
    else { Ain = (const u16*)(lay + OFF_H2); Cout = (layer == 0) ? (u16*)(P.ws + WS_X3) : P.out; }
    ldA = C::KTOT; ldC = C::LDW;
    rsrc_t srdW = mk_rsrc(stage_w<C>(P), (unsigned)(C::KTOT * C::LDW * 2));
    rsrc_t srdA = mk_rsrc(Ain, (unsigned)(S_ * C::KTOT * 2));
    rsrc_t srdC = mk_rsrc(Cout, (unsigned)(S_ * C::LDW * 2));

    const int wm = w % C::WM, wn = w / C::WM;
    const int fr = l & 15, fq = l >> 4;
    const int q = (l & 15) >> 2, p = l & 3;
    const int bh = ((q >> 1) & 1) | ((fq & 1) << 1);
    const int a_swz = (fr >> 1) & 7;
    const int erow = r0 + wm * C::MT * 16 + fr;
    const int ecol = n0 + wn * C::NT * 16 + 4 * fq;

    u32x2 res0[C::MT][C::NT], res1[C::MT][C::NT];
    f32x2 st_in[C::MT];
    if constexpr (C::EPI == EP_S2 || C::EPI == EP_S4 || C::EPI == EP_S6) {
        const u16* r0p = (C::EPI == EP_S2) ? xres : (const u16*)(lay + OFF_X2);
        rsrc_t srdR0 = mk_rsrc(r0p, (unsigned)(S_ * D_ * 2));
#pragma unroll
        for (int mt = 0; mt < C::MT; ++mt)
#pragma unroll
            for (int nt = 0; nt < C::NT; ++nt)
                res0[mt][nt] = __builtin_bit_cast(u32x2, __builtin_amdgcn_raw_buffer_load_b64(srdR0, (unsigned)(((erow + 16 * mt) * D_ + ecol + 16 * nt) * 2), 0, SC1AUX));
        if constexpr (C::EPI == EP_S6) {
            rsrc_t srdR1 = mk_rsrc(lay + OFF_X1, (unsigned)(S_ * D_ * 2));
#pragma unroll
            for (int mt = 0; mt < C::MT; ++mt)
#pragma unroll
                for (int nt = 0; nt < C::NT; ++nt)
                    res1[mt][nt] = __builtin_bit_cast(u32x2, __builtin_amdgcn_raw_buffer_load_b64(srdR1, (unsigned)(((erow + 16 * mt) * D_ + ecol + 16 * nt) * 2), 0, SC1AUX));
        }
        if constexpr (C::EPI == EP_S4) {
            rsrc_t srdS = mk_rsrc(lay + OFF_ST, (unsigned)(S_ * 8));
#pragma unroll
            for (int mt = 0; mt < C::MT; ++mt) {
                st_in[mt] = __builtin_bit_cast(f32x2, __builtin_amdgcn_raw_buffer_load_b64(srdS, (unsigned)((erow + 16 * mt) * 8), 0, SC1AUX));
            }
        }
    }

    f32x4 cs_in[C::NT];
    if constexpr (C::LN) {
        const float* cst = (const float*)(P.ws + WS_CS) + ((C::EPI == EP_S3) ? CS_W1 : CS_W3);
        rsrc_t srdCS = mk_rsrc(cst, (unsigned)(C::LDW * 4));
#pragma unroll
        for (int nt = 0; nt < C::NT; ++nt)
            cs_in[nt] = __builtin_bit_cast(f32x4, __builtin_amdgcn_raw_buffer_load_b128(srdCS, (unsigned)((ecol + 16 * nt) * 4), 0, 0));
    }
    unsigned bvoff[C::BPW];
    g_bvoff<C>(bvoff, n0, w, l);
    const int arow = 8 * w + (l >> 3);
    const int alu = (l & 7) ^ ((arow >> 1) & 7);
    const unsigned avoff = (unsigned)(((r0 + arow) * C::KTOT + alu * 8) * 2);
    auto issueA = [&](auto KC) {
        constexpr int kc = decltype(KC)::value;
        const unsigned dst = lds0 + (unsigned)((kc % C::D) * C::SLOT + w * 1024);
        if constexpr (C::AMODE == AM_SHIFT) {
            const int k0 = kc * 64 + alu * 8;
            const int grp = k0 / 112;
            const int sh = (grp == 0) ? -1 : (grp == 1) ? 1 : (grp == 2) ? -2 : 2;
            const int srow = r0 + arow + sh;
            const unsigned vo = (srow < 0 || srow >= S_) ? 0xFFFFFFF0u : (unsigned)((srow * C::KTOT + alu * 8) * 2);
            dma16<SC1DMA>(srdA, vo, (unsigned)(kc * 128), dst);
        } else {
            dma16<SC1DMA>(srdA, avoff, (unsigned)(kc * 128), dst);
        }
    };
    auto issueB = [&](auto KC) { g_issueB<C>(srdW, bvoff, decltype(KC)::value, lds0, w); };

    sfor<0, (C::D < C::NCHUNK ? C::D : C::NCHUNK)>([&](auto KC) { issueA(KC); });

    f32x4 acc[C::MT][C::NT];
#pragma unroll
    for (int mt = 0; mt < C::MT; ++mt)
#pragma unroll
        for (int nt = 0; nt < C::NT; ++nt) acc[mt][nt] = (f32x4){0.f, 0.f, 0.f, 0.f};
    float ssum[C::MT], ssq[C::MT];
#pragma unroll
    for (int mt = 0; mt < C::MT; ++mt) { ssum[mt] = 0.f; ssq[mt] = 0.f; }

    const int a_off0 = (wm * C::MT * 16 + fr) * 128 + ((fq ^ a_swz) * 16);
    const int a_off1 = (wm * C::MT * 16 + fr) * 128 + (((4 + fq) ^ a_swz) * 16);
    int b_off[C::NT];
#pragma unroll
    for (int nt = 0; nt < C::NT; ++nt) b_off[nt] = (8 * fq + q) * (2 * C::BN) + (((wn * C::NT + nt) ^ bh) * 32) + p * 8;

    ring_loop<C::NCHUNK, C::D, C::APW, C::BPW>(issueA, issueB, [&](auto KC) {
        constexpr int kc = decltype(KC)::value;
        const char* cA = smem + (kc % C::D) * C::SLOT;
        const char* cB = cA + C::A_BYTES;
#pragma unroll
        for (int s = 0; s < 2; ++s) {
            bf16x8 xa[C::MT], wb[C::NT];
#pragma unroll
            for (int mt = 0; mt < C::MT; ++mt) xa[mt] = *(const bf16x8*)(cA + (s ? a_off1 : a_off0) + mt * 2048);
#pragma unroll
            for (int nt = 0; nt < C::NT; ++nt)
                wb[nt] = ld_tr(cB + b_off[nt] + s * (32 * 2 * C::BN), cB + b_off[nt] + s * (32 * 2 * C::BN) + 4 * 2 * C::BN);
#pragma unroll
            for (int mt = 0; mt < C::MT; ++mt)
#pragma unroll
                for (int nt = 0; nt < C::NT; ++nt)
                    acc[mt][nt] = __builtin_amdgcn_mfma_f32_16x16x32_bf16(wb[nt], xa[mt], acc[mt][nt], 0, 0, 0);
            if constexpr (C::LN) {
                if (wn == ((2 * kc + s) % C::WN)) {
#pragma unroll
                    for (int mt = 0; mt < C::MT; ++mt) stat_acc(xa[mt], ssum[mt], ssq[mt]);
                }
            }
        }
    });

    float mean[C::MT], rstd[C::MT];
    if constexpr (C::LN) {
        f32x2* sst = (f32x2*)(smem + C::D * C::SLOT);
#pragma unroll
        for (int mt = 0; mt < C::MT; ++mt) {
            float s = grp_reduce(ssum[mt]), ss = grp_reduce(ssq[mt]);
            if (fq == 0) { f32x2 t; t[0] = s; t[1] = ss; sst[wn * C::BM + wm * C::MT * 16 + mt * 16 + fr] = t; }
        }
        wg_barrier();
#pragma unroll
        for (int mt = 0; mt < C::MT; ++mt) {
            float s = 0.f, ss = 0.f;
#pragma unroll
            for (int i = 0; i < C::WN; ++i) { f32x2 t = sst[i * C::BM + wm * C::MT * 16 + mt * 16 + fr]; s += t[0]; ss += t[1]; }
            float m = s * (1.f / D_);
            float v = fmaxf(ss * (1.f / D_) - m * m, 0.f);
            mean[mt] = m;
            if constexpr (C::EPI == EP_S3) {
                rstd[mt] = __builtin_amdgcn_rsqf(v * (1.f + LN_EPS) + LN_EPS * LN_EPS);
                if (tn == 0 && wn == 0 && fq == 0) {
                    rsrc_t srdS = mk_rsrc(lay + OFF_ST, (unsigned)(S_ * 8));
                    f32x2 sv; sv[0] = m; sv[1] = __builtin_amdgcn_rsqf(v + LN_EPS);
                    __builtin_amdgcn_raw_buffer_store_b64(__builtin_bit_cast(u32x2, sv), srdS, (unsigned)((erow + 16 * mt) * 8), 0, SC1AUX);
                }
            } else {
                rstd[mt] = __builtin_amdgcn_rsqf(v + LN_EPS);
            }
        }
    }
    u32x2 ov[C::MT][C::NT];
#pragma unroll
    for (int nt = 0; nt < C::NT; ++nt) {
#pragma unroll
        for (int mt = 0; mt < C::MT; ++mt) {
            float v[4];
#pragma unroll
            for (int r = 0; r < 4; ++r) v[r] = acc[mt][nt][r];
            u32x2 o;
            if constexpr (C::EPI == EP_S3 || C::EPI == EP_S5) {
                const f32x4 cs = cs_in[nt];
#pragma unroll
                for (int r = 0; r < 4; ++r) v[r] = gelu_tanh(rbf(rstd[mt] * (v[r] - mean[mt] * cs[r])));
                o[0] = pack2(v[0], v[1]); o[1] = pack2(v[2], v[3]);
            } else if constexpr (C::EPI == EP_S2) {
                u32x2 x = res0[mt][nt];
                o[0] = pack2(lo_f(x[0]) + rbf(v[0]), hi_f(x[0]) + rbf(v[1]));
                o[1] = pack2(lo_f(x[1]) + rbf(v[2]), hi_f(x[1]) + rbf(v[3]));
            } else if constexpr (C::EPI == EP_S4) {
                u32x2 x = res0[mt][nt];
                const float m = st_in[mt][0], rs = st_in[mt][1];
                o[0] = pack2(rbf((lo_f(x[0]) - m) * rs) + rbf(v[0]), rbf((hi_f(x[0]) - m) * rs) + rbf(v[1]));
                o[1] = pack2(rbf((lo_f(x[1]) - m) * rs) + rbf(v[2]), rbf((hi_f(x[1]) - m) * rs) + rbf(v[3]));
            } else {
                u32x2 x = res0[mt][nt], y = res1[mt][nt];
                o[0] = pack2(lo_f(x[0]) + rbf(lo_f(y[0]) + rbf(v[0])), hi_f(x[0]) + rbf(hi_f(y[0]) + rbf(v[1])));
                o[1] = pack2(lo_f(x[1]) + rbf(lo_f(y[1]) + rbf(v[2])), hi_f(x[1]) + rbf(hi_f(y[1]) + rbf(v[3])));
            }
            ov[mt][nt] = o;
        }
    }
#pragma unroll
    for (int mt = 0; mt < C::MT; ++mt)
#pragma unroll
        for (int nt = 0; nt < C::NT; nt += 2) {
            auto r0 = __builtin_amdgcn_permlane16_swap(ov[mt][nt][0], ov[mt][nt + 1][0], false, false);
            auto r1 = __builtin_amdgcn_permlane16_swap(ov[mt][nt][1], ov[mt][nt + 1][1], false, false);
            u32x4 o4; o4[0] = r0[0]; o4[1] = r1[0]; o4[2] = r0[1]; o4[3] = r1[1];
            const int col = n0 + wn * C::NT * 16 + 16 * (nt + (fq & 1)) + 8 * (fq >> 1);
            __builtin_amdgcn_raw_buffer_store_b128(o4, srdC, (unsigned)(((erow + 16 * mt) * C::LDW + col) * 2), 0, SC1AUX);
        }
}

struct S1C {
    static constexpr int BM = 80, BN = 192, NCHUNK = D_ / 64, D = 3, BPW = 3;
    static constexpr int A_BYTES = BM * 128, B_BYTES = 64 * BN * 2, SLOT = A_BYTES + B_BYTES;
    static constexpr int QS = 392;
    static constexpr int STG_BYTES = BM * QS;
    static constexpr int LDS_TOP = 163840;
    static constexpr int STG_OFF = LDS_TOP - STG_BYTES;
    static constexpr int STAT_OFF = STG_OFF - 4 * 80 * 8;
};

__device__ __forceinline__ void s1_bvoff(unsigned (&bvoff)[3], int hp, int w, int l) {
#pragma unroll
    for (int i = 0; i < 3; ++i) {
        int u = 64 * (w * 3 + i) + l;
        int k = u / 24, pu = u % 24;
        int h = ((k >> 1) & 1) | (((k >> 3) & 1) << 1);
        int lu32 = (pu >> 1) ^ h;
        int c = lu32 * 16 + (pu & 1) * 8;
        int gcol = (c >> 6) * D_ + hp * 56 + (c & 63);
        bvoff[i] = (unsigned)((k * (3 * D_) + gcol) * 2);
    }
}
__device__ __forceinline__ void s1_issueB(rsrc_t srdW, const unsigned (&bvoff)[3], int kc, unsigned lds0, int w) {
#pragma unroll
    for (int i = 0; i < 3; ++i)
        dma16<0>(srdW, bvoff[i], (unsigned)(kc * 64 * 3 * D_ * 2), lds0 + (unsigned)((kc % S1C::D) * S1C::SLOT + S1C::A_BYTES + (w * 3 + i) * 1024));
}
__device__ __forceinline__ void s1_prefetch(const Params& P, int tile, unsigned lds0) {
    const int w = __builtin_amdgcn_readfirstlane(threadIdx.x >> 6), l = threadIdx.x & 63;
    rsrc_t srdW = mk_rsrc(P.wqkv, (unsigned)(D_ * 3 * D_ * 2));
    unsigned bvoff[3];
    s1_bvoff(bvoff, tile & 7, w, l);
#pragma unroll
    for (int j = 0; j < S1C::D; ++j) s1_issueB(srdW, bvoff, j, lds0, w);
}

template <int APW, int MT>
__device__ __forceinline__ void s1_gemm(const Params& P, const u16* xin, int r0, int hp, char* smem, int w, int l) {
    const unsigned lds0 = (unsigned)(size_t)smem;
    rsrc_t srdW = mk_rsrc(P.wqkv, (unsigned)(D_ * 3 * D_ * 2));
    rsrc_t srdA = mk_rsrc(xin, (unsigned)(S_ * D_ * 2));
    const int ng = w & 3, mg = w >> 2;
    const int mt0 = mg * 3;
    const int fr = l & 15, fq = l >> 4, q = (l & 15) >> 2, p = l & 3;
    const int bh = ((q >> 1) & 1) | ((fq & 1) << 1);
    const int a_swz = (fr >> 1) & 7;
    unsigned bvoff[3];
    s1_bvoff(bvoff, hp, w, l);
    unsigned avoff[APW];
#pragma unroll
    for (int i = 0; i < APW; ++i) {
        const int j = (i == 0) ? w : 8 + w;
        const int arow = 8 * j + (l >> 3);
        const int alu = (l & 7) ^ ((arow >> 1) & 7);
        const int grow = r0 - 16 + arow;
        avoff[i] = (grow < 0) ? 0xFFFFFFF0u : (unsigned)((grow * D_ + alu * 8) * 2);
    }
    auto issueA = [&](auto KC) {
        constexpr int kc = decltype(KC)::value;
#pragma unroll
        for (int i = 0; i < APW; ++i) {
            const int j = (i == 0) ? w : 8 + w;
            dma16<SC1DMA>(srdA, avoff[i], (unsigned)(kc * 128), lds0 + (unsigned)((kc % S1C::D) * S1C::SLOT + j * 1024));
        }
    };
    auto issueB = [&](auto KC) { s1_issueB(srdW, bvoff, decltype(KC)::value, lds0, w); };
    sfor<0, S1C::D>([&](auto KC) { issueA(KC); });

    f32x4 acc[MT][3];
#pragma unroll
    for (int mt = 0; mt < MT; ++mt)
#pragma unroll
        for (int nt = 0; nt < 3; ++nt) acc[mt][nt] = (f32x4){0.f, 0.f, 0.f, 0.f};
    float ssum[MT], ssq[MT];
#pragma unroll
    for (int mt = 0; mt < MT; ++mt) { ssum[mt] = 0.f; ssq[mt] = 0.f; }
    f32x4 cs_in[3];
    {
        rsrc_t srdCS = mk_rsrc(P.ws + WS_CS, (unsigned)(1344 * 4));
#pragma unroll
        for (int nt = 0; nt < 3; ++nt) {
            const int c = (ng * 3 + nt) * 16 + 4 * fq;
            const int gcol = (c >> 6) * D_ + hp * 56 + (c & 63);
            cs_in[nt] = __builtin_bit_cast(f32x4, __builtin_amdgcn_raw_buffer_load_b128(srdCS, (unsigned)(gcol * 4), 0, 0));
        }
    }
    const int a_off0 = (mt0 * 16 + fr) * 128 + ((fq ^ a_swz) * 16);
    const int a_off1 = (mt0 * 16 + fr) * 128 + (((4 + fq) ^ a_swz) * 16);
    int b_off[3];
#pragma unroll
    for (int nt = 0; nt < 3; ++nt) b_off[nt] = (8 * fq + q) * (2 * S1C::BN) + (((ng * 3 + nt) ^ bh) * 32) + p * 8;

    ring_loop<S1C::NCHUNK, S1C::D, APW, 3>(issueA, issueB, [&](auto KC) {
        constexpr int kc = decltype(KC)::value;
        const char* cA = smem + (kc % S1C::D) * S1C::SLOT;
        const char* cB = cA + S1C::A_BYTES;
#pragma unroll
        for (int s = 0; s < 2; ++s) {
            bf16x8 xa[MT], wb[3];
#pragma unroll
            for (int mt = 0; mt < MT; ++mt) xa[mt] = *(const bf16x8*)(cA + (s ? a_off1 : a_off0) + mt * 2048);
#pragma unroll
            for (int nt = 0; nt < 3; ++nt)
                wb[nt] = ld_tr(cB + b_off[nt] + s * (32 * 2 * S1C::BN), cB + b_off[nt] + s * (32 * 2 * S1C::BN) + 4 * 2 * S1C::BN);
#pragma unroll
            for (int mt = 0; mt < MT; ++mt)
#pragma unroll
                for (int nt = 0; nt < 3; ++nt)
                    acc[mt][nt] = __builtin_amdgcn_mfma_f32_16x16x32_bf16(wb[nt], xa[mt], acc[mt][nt], 0, 0, 0);
            if (ng == ((2 * kc + s) & 3)) {
#pragma unroll
                for (int mt = 0; mt < MT; ++mt) stat_acc(xa[mt], ssum[mt], ssq[mt]);
            }
        }
    });
    char* stg = smem + S1C::STG_OFF;
    f32x2* sst = (f32x2*)(smem + S1C::STAT_OFF);
#pragma unroll
    for (int mt = 0; mt < MT; ++mt) {
        float s = grp_reduce(ssum[mt]), ss = grp_reduce(ssq[mt]);
        if (fq == 0) { f32x2 t; t[0] = s; t[1] = ss; sst[ng * 80 + (mt0 + mt) * 16 + fr] = t; }
    }
    wg_barrier();
    float mean[MT], rstd[MT];
#pragma unroll
    for (int mt = 0; mt < MT; ++mt) {
        float s = 0.f, ss = 0.f;
#pragma unroll
        for (int i = 0; i < 4; ++i) { f32x2 t = sst[i * 80 + (mt0 + mt) * 16 + fr]; s += t[0]; ss += t[1]; }
        float m = s * (1.f / D_);
        float v = fmaxf(ss * (1.f / D_) - m * m, 0.f);
        mean[mt] = m; rstd[mt] = __builtin_amdgcn_rsqf(v + LN_EPS);
    }
#pragma unroll
    for (int nt = 0; nt < 3; ++nt) {
        const f32x4 cs = cs_in[nt];
#pragma unroll
        for (int mt = 0; mt < MT; ++mt) {
            u32x2 o;
            o[0] = pack2(rstd[mt] * (acc[mt][nt][0] - mean[mt] * cs[0]), rstd[mt] * (acc[mt][nt][1] - mean[mt] * cs[1]));
            o[1] = pack2(rstd[mt] * (acc[mt][nt][2] - mean[mt] * cs[2]), rstd[mt] * (acc[mt][nt][3] - mean[mt] * cs[3]));
            *(u32x2*)(stg + ((mt0 + mt) * 16 + fr) * S1C::QS + ((ng * 3 + nt) * 16 + 4 * fq) * 2) = o;
        }
    }
}

__device__ __forceinline__ void s1_attention(int r0, int hp, char* smem, u16* oout, int tid) {
    const char* stg = smem + S1C::STG_OFF;
    const int row = tid >> 2, hh = (tid >> 1) & 1, half = tid & 1;
    const int cb = (hh * 28 + half * 16) * 2;
    u32x2 qv[4];
    {
        const char* qp = stg + (16 + row) * S1C::QS + cb;
#pragma unroll
        for (int i = 0; i < 4; ++i) qv[i] = *(const u32x2*)(qp + i * 8);
        if (half) { qv[3][0] = 0u; qv[3][1] = 0u; }
    }
    float lg[8];
#pragma unroll
    for (int wd = 0; wd < 8; ++wd) {
        const char* kp = stg + (16 + row - 2 * wd) * S1C::QS + 128 + cb;
        float a = 0.f;
#pragma unroll
        for (int i = 0; i < 4; ++i) {
            u32x2 kv = *(const u32x2*)(kp + i * 8);
            a = dot2(qv[i][0], kv[0], a);
            a = dot2(qv[i][1], kv[1], a);
        }
        a += __shfl_xor(a, 1);
        float v = rbf(rbf(a) * 0.18898223650461362f);
        lg[wd] = (r0 + row - 2 * wd < 0) ? -1e9f : v;
    }
    float mx = lg[0];
#pragma unroll
    for (int wd = 1; wd < 8; ++wd) mx = fmaxf(mx, lg[wd]);
    float e[8], sum = 0.f;
#pragma unroll
    for (int wd = 0; wd < 8; ++wd) { e[wd] = __expf(lg[wd] - mx); sum += e[wd]; }
    const float inv = __builtin_amdgcn_rcpf(sum);
    float o[16];
#pragma unroll
    for (int i = 0; i < 16; ++i) o[i] = 0.f;
#pragma unroll
    for (int wp = 0; wp < 4; ++wp) {
        const unsigned pp = pack2(e[2 * wp] * inv, e[2 * wp + 1] * inv);
        const char* v0 = stg + (16 + row - 4 * wp) * S1C::QS + 256 + cb;
        const char* v1 = stg + (16 + row - 4 * wp - 2) * S1C::QS + 256 + cb;
#pragma unroll
        for (int i = 0; i < 4; ++i) {
            u32x2 a = *(const u32x2*)(v0 + i * 8), b = *(const u32x2*)(v1 + i * 8);
#pragma unroll
            for (int j = 0; j < 2; ++j) {
                unsigned lo = (a[j] & 0xffffu) | (b[j] << 16);
                unsigned hi = (a[j] >> 16) | (b[j] & 0xffff0000u);
                o[4 * i + 2 * j] = dot2(lo, pp, o[4 * i + 2 * j]);
                o[4 * i + 2 * j + 1] = dot2(hi, pp, o[4 * i + 2 * j + 1]);
            }
        }
    }
    rsrc_t srdO = mk_rsrc(oout, (unsigned)(S_ * D_ * 2));
    const unsigned ob = (unsigned)(((r0 + row) * D_ + hp * 56 + hh * 28 + half * 16) * 2);
#pragma unroll
    for (int i = 0; i < 4; ++i) {
        if (i < 3 || !half) {
            u32x2 ov; ov[0] = pack2(o[4 * i], o[4 * i + 1]); ov[1] = pack2(o[4 * i + 2], o[4 * i + 3]);
            __builtin_amdgcn_raw_buffer_store_b64(ov, srdO, ob + i * 8, 0, SC1AUX);
        }
    }
}

__device__ __forceinline__ void s1_main(const Params& P, int layer, int tile, char* smem) {
    const int tid = threadIdx.x, w = __builtin_amdgcn_readfirstlane(tid >> 6), l = tid & 63;
    const int r0 = (tile >> 3) * 64, hp = tile & 7;
    const u16* xin = (layer == 0) ? P.x : (const u16*)(P.ws + WS_X3);
    u16* oout = (u16*)(P.ws + WS_L0 + (size_t)layer * LAYER_BYTES + OFF_O);
    if (w < 2) s1_gemm<2, 3>(P, xin, r0, hp, smem, w, l);
    else if (w < 4) s1_gemm<1, 3>(P, xin, r0, hp, smem, w, l);
    else s1_gemm<1, 2>(P, xin, r0, hp, smem, w, l);
    wg_barrier();
    if (tid < 256) s1_attention(r0, hp, smem, oout, tid);
}

__device__ __forceinline__ int xcd_tile(int b, int ntn) {
    const int xcd = b & 7, idx = b >> 3;
    const int panel = xcd * 4 + idx / ntn, tn = idx % ntn;
    return panel * ntn + tn;
}

__global__ __launch_bounds__(512) void k_prep(Params P) {
    __shared__ float red[64][65];
    const int b = blockIdx.x, t = threadIdx.x;
    {
        const int gid = b * 512 + t;
        if (gid < 16 * 32) ((unsigned*)(P.ws + WS_CNT))[gid * 64] = 0u;
    }
    const u16* W; int ldw, c0, o0;
    if (b < 21) { W = P.wqkv; ldw = 3 * D_; c0 = b * 64; o0 = CS_QKV + c0; }
    else if (b < 28) { W = P.w1; ldw = D_; c0 = (b - 21) * 64; o0 = CS_W1 + c0; }
    else { W = P.w3; ldw = F_; c0 = (b - 28) * 64; o0 = CS_W3 + c0; }
    const int cc = (t & 7) * 8, rg = t >> 3;
    u32x4 v[7];
#pragma unroll
    for (int i = 0; i < 7; ++i) v[i] = *(const u32x4*)(W + (size_t)(rg + 64 * i) * ldw + c0 + cc);
    float a[8];
#pragma unroll
    for (int j = 0; j < 8; ++j) a[j] = 0.f;
#pragma unroll
    for (int i = 0; i < 7; ++i)
#pragma unroll
        for (int j = 0; j < 4; ++j) { a[2 * j] += lo_f(v[i][j]); a[2 * j + 1] += hi_f(v[i][j]); }
#pragma unroll
    for (int j = 0; j < 8; ++j) red[rg][cc + j] = a[j];
    __syncthreads();
    if (t < 64) {
        float s = 0.f;
        for (int i = 0; i < 64; ++i) s += red[i][t];
        ((float*)(P.ws + WS_CS))[o0 + t] = s;
    }
}

__device__ __forceinline__ unsigned* cnt_ptr(const Params& P, int sidx, int panel) {
    return (unsigned*)(P.ws + WS_CNT) + (sidx * 32 + panel) * 64;
}
__device__ __forceinline__ void wait_cnt(const Params& P, int sidx, int p0, int p1, unsigned target) {
    if (threadIdx.x == 0) {
        for (int p = p0; p <= p1; ++p) {
            if (p < 0 || p >= 32) continue;
            unsigned* c = cnt_ptr(P, sidx, p);
            unsigned spins = 0;
            while (__hip_atomic_load(c, __ATOMIC_RELAXED, __HIP_MEMORY_SCOPE_AGENT) < target) {
                __builtin_amdgcn_s_sleep(1);
                if (++spins > (1u << 24)) break;
            }
        }
    }
    wg_barrier();
}
__device__ __forceinline__ void signal_cnt(const Params& P, int sidx, int panel) {
    wg_barrier();
    if (threadIdx.x == 0) __hip_atomic_fetch_add(cnt_ptr(P, sidx, panel), 1u, __ATOMIC_RELAXED, __HIP_MEMORY_SCOPE_AGENT);
}

template <class C> __device__ __forceinline__ void g_stage(const Params& P, int layer, int b, char* smem, bool active,
                                                            int dep_s, int dp0, int dp1, unsigned dep_t) {
    if (!active) return;
    const int tile = xcd_tile(b, C::NTN);
    wait_cnt(P, dep_s, (tile / C::NTN) + dp0, (tile / C::NTN) + dp1, dep_t);
    gemm_main<C>(P, layer, tile, smem);
}

__global__ __launch_bounds__(NT_, 2) void k_persist(Params P) {
    __shared__ __attribute__((aligned(16))) char smem[163840];
    const unsigned lds0 = (unsigned)(size_t)smem;
    const int b = blockIdx.x;
    const bool act = (b >> 3) < 28;
    const int t1 = xcd_tile(b, 8);
    s1_prefetch(P, t1, lds0);
    for (int layer = 0; layer < 2; ++layer) {
        const int sb = layer * 6;
        if (layer == 1) wait_cnt(P, 5, (t1 >> 3) - 1, (t1 >> 3), 7u);
        s1_main(P, layer, t1, smem);
        wg_barrier();
        if (act) gemm_prefetch<GCfg<EP_S2>>(P, xcd_tile(b, 7), lds0);
        if (act) wait_vm<GCfg<EP_S2>::D * GCfg<EP_S2>::BPW>(); else wait_vm<0>();
        signal_cnt(P, sb + 0, t1 >> 3);
        if (act) {
            g_stage<GCfg<EP_S2>>(P, layer, b, smem, true, sb + 0, 0, 0, 8u);
            wg_barrier();
            gemm_prefetch<GCfg<EP_S3>>(P, xcd_tile(b, 7), lds0);
            wait_vm<GCfg<EP_S3>::D * GCfg<EP_S3>::BPW>();
            signal_cnt(P, sb + 1, xcd_tile(b, 7) / 7);
            g_stage<GCfg<EP_S3>>(P, layer, b, smem, true, sb + 1, 0, 0, 7u);
            wg_barrier();
            gemm_prefetch<GCfg<EP_S4>>(P, xcd_tile(b, 7), lds0);
            wait_vm<GCfg<EP_S4>::D * GCfg<EP_S4>::BPW>();
            signal_cnt(P, sb + 2, xcd_tile(b, 7) / 7);
            g_stage<GCfg<EP_S4>>(P, layer, b, smem, true, sb + 2, -1, 1, 7u);
            wg_barrier();
            gemm_prefetch<GCfg<EP_S5>>(P, xcd_tile(b, 7), lds0);
            wait_vm<GCfg<EP_S5>::D * GCfg<EP_S5>::BPW>();
            signal_cnt(P, sb + 3, xcd_tile(b, 7) / 7);
            g_stage<GCfg<EP_S5>>(P, layer, b, smem, true, sb + 3, 0, 0, 7u);
            wg_barrier();
            gemm_prefetch<GCfg<EP_S6>>(P, xcd_tile(b, 7), lds0);
            wait_vm<GCfg<EP_S6>::D * GCfg<EP_S6>::BPW>();
            signal_cnt(P, sb + 4, xcd_tile(b, 7) / 7);
            g_stage<GCfg<EP_S6>>(P, layer, b, smem, true, sb + 4, 0, 0, 7u);
            wg_barrier();
            if (layer == 0) { s1_prefetch(P, t1, lds0); wait_vm<S1C::D * S1C::BPW>(); } else wait_vm<0>();
            signal_cnt(P, sb + 5, xcd_tile(b, 7) / 7);
        } else if (layer == 0) {
            s1_prefetch(P, t1, lds0);
        }
    }
}

}

extern "C" void kernel_launch(void* const* d_in, const int* in_sizes, int n_in,
                              void* d_out, int out_size, void* d_ws, size_t ws_size,
                              hipStream_t stream) {
    Params P;
    P.x = (const u16*)d_in[0]; P.wqkv = (const u16*)d_in[1]; P.wo = (const u16*)d_in[2];
    P.w1 = (const u16*)d_in[4]; P.w2 = (const u16*)d_in[5]; P.w3 = (const u16*)d_in[6]; P.w4 = (const u16*)d_in[7];
    P.out = (u16*)d_out; P.ws = (char*)d_ws;
    k_prep<<<56, 512, 0, stream>>>(P);
    k_persist<<<256, NT_, 0, stream>>>(P);

}
```

```cpp
#include <hip/hip_runtime.h>
#include <hip/hip_bf16.h>
#include <stdint.h>
#include <type_traits>

#define SC1AUX 16
#define SC1DMA 1
namespace {

typedef unsigned short u16;
typedef __attribute__((ext_vector_type(8))) short bf16x8;
typedef __attribute__((ext_vector_type(4))) short s16x4;
typedef __attribute__((ext_vector_type(4))) float f32x4;
typedef __attribute__((ext_vector_type(4))) unsigned u32x4;
typedef __attribute__((ext_vector_type(2))) unsigned u32x2;
typedef __attribute__((ext_vector_type(2))) float f32x2;
typedef __attribute__((ext_vector_type(2))) __bf16 bf2_t;
typedef __attribute__((address_space(3))) s16x4 lds_s16x4;

constexpr int S_ = 2048, D_ = 448, F_ = 1792;
constexpr int NT_ = 512;
constexpr float LN_EPS = 1e-5f;

constexpr size_t WS_CNT = 0;
constexpr size_t CNT_BYTES = 16 * 32 * 256;
constexpr size_t SD2 = (size_t)S_ * D_ * 2;
constexpr size_t WS_L0 = CNT_BYTES;
constexpr size_t LAYER_BYTES = 4 * SD2 + (size_t)S_ * 8 + (size_t)S_ * F_ * 2;
constexpr size_t OFF_O = 0, OFF_X2 = SD2, OFF_H = 2 * SD2, OFF_X1 = 3 * SD2, OFF_ST = 4 * SD2, OFF_H2 = 4 * SD2 + (size_t)S_ * 8;
constexpr size_t WS_X3 = WS_L0 + 2 * LAYER_BYTES;
constexpr size_t WS_CS = WS_X3 + SD2;
constexpr size_t CS_QKV = 0, CS_W1 = 1344, CS_W3 = 1344 + 448;
constexpr int NCS = 1344 + 448 + 1792;

struct Params {
    const u16* x; const u16* wqkv; const u16* wo; const u16* w1; const u16* w2; const u16* w3; const u16* w4;
    u16* out; char* ws;
};

__device__ __forceinline__ float bf2f(u16 h) { return __builtin_bit_cast(float, (unsigned)h << 16); }
__device__ __forceinline__ u16 f2bf(float f) { return __builtin_bit_cast(u16, __float2bfloat16(f)); }
__device__ __forceinline__ float rbf(float f) { return bf2f(f2bf(f)); }
__device__ __forceinline__ unsigned pack2(float lo, float hi) { return (unsigned)f2bf(lo) | ((unsigned)f2bf(hi) << 16); }
__device__ __forceinline__ float lo_f(unsigned u) { return __builtin_bit_cast(float, u << 16); }
__device__ __forceinline__ float hi_f(unsigned u) { return __builtin_bit_cast(float, u & 0xffff0000u); }
__device__ __forceinline__ float dot2(unsigned a, unsigned b, float c) {
    return __builtin_amdgcn_fdot2_f32_bf16(__builtin_bit_cast(bf2_t, a), __builtin_bit_cast(bf2_t, b), c, false);
}
__device__ __forceinline__ float gelu_tanh(float u) {
    float t = 0.7978845608028654f * (u + 0.044715f * u * u * u);
    float e = __builtin_amdgcn_exp2f(-2.885390081777927f * t);
    return u * __builtin_amdgcn_rcpf(1.f + e);
}

__device__ __forceinline__ unsigned ln_gelu_pk(f32x2 a, f32x2 cs, float mean, float rstd) {
    f32x2 v = (a - cs * mean) * rstd;
    unsigned p = pack2(v[0], v[1]);
    f32x2 u; u[0] = lo_f(p); u[1] = hi_f(p);
    f32x2 arg = u * (u * u * -0.10294324f + -2.30220820f);
    f32x2 e; e[0] = __builtin_amdgcn_exp2f(arg[0]); e[1] = __builtin_amdgcn_exp2f(arg[1]);
    f32x2 d = e + 1.f;
    f32x2 r; r[0] = __builtin_amdgcn_rcpf(d[0]); r[1] = __builtin_amdgcn_rcpf(d[1]);
    f32x2 g = u * r;
    return pack2(g[0], g[1]);
}
template <int N> __device__ __forceinline__ void wait_vm() { asm volatile("s_waitcnt vmcnt(%0)" :: "n"(N) : "memory"); }
__device__ __forceinline__ void wg_barrier() {
    asm volatile("s_waitcnt lgkmcnt(0)" ::: "memory");
    __builtin_amdgcn_s_barrier();
    asm volatile("" ::: "memory");
}
template <int I, int N, class F> __device__ __forceinline__ void sfor(F&& f) {
    if constexpr (I < N) { f(std::integral_constant<int, I>{}); sfor<I + 1, N>(f); }
}

typedef __amdgpu_buffer_rsrc_t rsrc_t;
__device__ __forceinline__ rsrc_t mk_rsrc(const void* p, unsigned bytes) {
    return __builtin_amdgcn_make_buffer_rsrc((void*)p, (short)0, bytes, 0x00020000);
}
template <int SC1>
__device__ __forceinline__ void dma16(rsrc_t srd, unsigned voff, unsigned soff, unsigned lds_base) {
    if constexpr (SC1)
        asm volatile("s_mov_b32 m0, %0\n\ts_nop 0\n\tbuffer_load_dwordx4 %1, %2, %3 offen sc1 lds" :: "s"(lds_base), "v"(voff), "s"(srd), "s"(soff) : "memory");
    else
        asm volatile("s_mov_b32 m0, %0\n\ts_nop 0\n\tbuffer_load_dwordx4 %1, %2, %3 offen lds" :: "s"(lds_base), "v"(voff), "s"(srd), "s"(soff) : "memory");
}
__device__ __forceinline__ bf16x8 ld_tr(const char* p0, const char* p1) {
    s16x4 lo = __builtin_amdgcn_ds_read_tr16_b64_v4i16((lds_s16x4*)(p0));
    s16x4 hi = __builtin_amdgcn_ds_read_tr16_b64_v4i16((lds_s16x4*)(p1));
    bf16x8 r; r[0] = lo[0]; r[1] = lo[1]; r[2] = lo[2]; r[3] = lo[3]; r[4] = hi[0]; r[5] = hi[1]; r[6] = hi[2]; r[7] = hi[3];
    return r;
}

template <int NCHUNK, int D, int APW, int BPW> constexpr int ring_nout(int kc) {
    int cnt = 0;
    if (kc < D) {
        cnt += (D - 1 - kc) * APW;
        for (int i = 1; i < kc; ++i) if (i - 1 + D < NCHUNK) cnt += APW + BPW;
    } else {
        for (int i = kc - D + 2; i < kc; ++i) if (i - 1 + D < NCHUNK) cnt += APW + BPW;
    }
    return cnt;
}
template <int NCHUNK, int D, int APW, int BPW, class IA, class IB, class CP>
__device__ __forceinline__ void ring_loop(IA&& issueA, IB&& issueB, CP&& compute) {
    sfor<0, NCHUNK>([&](auto KC) {
        constexpr int kc = decltype(KC)::value;
        wait_vm<ring_nout<NCHUNK, D, APW, BPW>(kc)>();
        __builtin_amdgcn_s_barrier();
        asm volatile("" ::: "memory");
        if constexpr (kc >= 1 && kc - 1 + D < NCHUNK) {
            issueA(std::integral_constant<int, kc - 1 + D>{});
            issueB(std::integral_constant<int, kc - 1 + D>{});
        }
        compute(KC);
    });
}

__device__ __forceinline__ void stat_acc(const bf16x8& f, float& s, float& ss) {
    u32x4 u = __builtin_bit_cast(u32x4, f);
#pragma unroll
    for (int i = 0; i < 4; ++i) { s = dot2(u[i], 0x3F803F80u, s); ss = dot2(u[i], u[i], ss); }
}
__device__ __forceinline__ void sum_acc(const bf16x8& f, float& s) {
    u32x4 u = __builtin_bit_cast(u32x4, f);
#pragma unroll
    for (int i = 0; i < 4; ++i) s = dot2(u[i], 0x3F803F80u, s);
}
__device__ __forceinline__ float grp_reduce(float v) {
    v += __shfl_xor(v, 16);
    v += __shfl_xor(v, 32);
    return v;
}

enum { AM_PLAIN = 0, AM_SHIFT = 1 };
enum { EP_S2 = 2, EP_S3 = 3, EP_S4 = 4, EP_S5 = 5, EP_S6 = 6 };

template <int EPI_> struct GCfg {
    static constexpr int EPI = EPI_;
    static constexpr int BM = 64;
    static constexpr int BN = (EPI_ == EP_S5) ? 256 : 64;
    static constexpr int KTOT = (EPI_ == EP_S6) ? F_ : D_;
    static constexpr int NCHUNK = KTOT / 64;
    static constexpr int D = (EPI_ == EP_S5) ? 4 : (EPI_ == EP_S6) ? 8 : 7;
    static constexpr int NTN = ((EPI_ == EP_S5) ? F_ : D_) / BN;
    static constexpr int LDW = (EPI_ == EP_S5) ? F_ : D_;
    static constexpr int MT = (EPI_ == EP_S5) ? 2 : 1;
    static constexpr int NT = (EPI_ == EP_S5) ? 4 : 2;
    static constexpr int WM = BM / 16 / MT;
    static constexpr int WN = 8 / WM;
    static constexpr int APW = BM / 64;
    static constexpr int BPW = BN / 64;
    static constexpr int A_BYTES = BM * 128, B_BYTES = 64 * BN * 2, SLOT = A_BYTES + B_BYTES;
    static constexpr bool LN = (EPI_ == EP_S3 || EPI_ == EP_S5);
    static constexpr int AMODE = (EPI_ == EP_S4) ? AM_SHIFT : AM_PLAIN;
    static constexpr int NTILES = (S_ / BM) * NTN;
};

template <class C>
__device__ __forceinline__ const u16* stage_w(const Params& P) {
    if constexpr (C::EPI == EP_S2) return P.wo;
    else if constexpr (C::EPI == EP_S3) return P.w1;
    else if constexpr (C::EPI == EP_S4) return P.w2;
    else if constexpr (C::EPI == EP_S5) return P.w3;
    else return P.w4;
}

template <class C>
__device__ __forceinline__ void g_issueB(rsrc_t srdW, const unsigned (&bvoff)[C::BPW], int kc, unsigned lds0, int w) {
#pragma unroll
    for (int i = 0; i < C::BPW; ++i)
        dma16<0>(srdW, bvoff[i], (unsigned)(kc * 64 * C::LDW * 2),
                 lds0 + (unsigned)((kc % C::D) * C::SLOT + C::A_BYTES + (w * C::BPW + i) * 1024));
}
template <class C>
__device__ __forceinline__ void g_bvoff(unsigned (&bvoff)[C::BPW], int n0, int w, int l) {
    constexpr int U = C::BN / 8;
#pragma unroll
    for (int i = 0; i < C::BPW; ++i) {
        int u = 64 * (w * C::BPW + i) + l;
        int k = u / U, pu = u % U;
        int h = (C::BN >= 128) ? ((k & 3) | (((k >> 3) & 1) << 2)) : (((k >> 1) & 1) | (((k >> 3) & 1) << 1));
        int lu32 = (pu >> 1) ^ h;
        bvoff[i] = (unsigned)((k * C::LDW + n0 + lu32 * 16 + (pu & 1) * 8) * 2);
    }
}

template <class C>
__device__ __forceinline__ void gemm_prefetch(const Params& P, int tile, unsigned lds0) {
    const int w = __builtin_amdgcn_readfirstlane(threadIdx.x >> 6), l = threadIdx.x & 63;
    const int n0 = (tile % C::NTN) * C::BN;
    rsrc_t srdW = mk_rsrc(stage_w<C>(P), (unsigned)(C::KTOT * C::LDW * 2));
    unsigned bvoff[C::BPW];
    g_bvoff<C>(bvoff, n0, w, l);
#pragma unroll
    for (int j = 0; j < C::D; ++j) g_issueB<C>(srdW, bvoff, j, lds0, w);
}

template <class C>
__device__ __forceinline__ void gemm_main(const Params& P, int layer, int tile, char* smem) {
    const unsigned lds0 = (unsigned)(size_t)smem;
    const int tid = threadIdx.x, w = __builtin_amdgcn_readfirstlane(tid >> 6), l = tid & 63;
    const int tm = tile / C::NTN, tn = tile % C::NTN;
    const int r0 = tm * C::BM, n0 = tn * C::BN;
    char* lay = P.ws + WS_L0 + (size_t)layer * LAYER_BYTES;
    const u16* xres = (layer == 0) ? P.x : (const u16*)(P.ws + WS_X3);
    const u16* Ain; u16* Cout; unsigned ldA, ldC;
    if constexpr (C::EPI == EP_S2) { Ain = (const u16*)(lay + OFF_O); Cout = (u16*)(lay + OFF_X2); }
    else if constexpr (C::EPI == EP_S3) { Ain = (const u16*)(lay + OFF_X2); Cout = (u16*)(lay + OFF_H); }
    else if constexpr (C::EPI == EP_S4) { Ain = (const u16*)(lay + OFF_H); Cout = (u16*)(lay + OFF_X1); }
    else if constexpr (C::EPI == EP_S5) { Ain = (const u16*)(lay + OFF_X1); Cout = (u16*)(lay + OFF_H2); }
    else { Ain = (const u16*)(lay + OFF_H2); Cout = (layer == 0) ? (u16*)(P.ws + WS_X3) : P.out; }
    ldA = C::KTOT; ldC = C::LDW;
    rsrc_t srdW = mk_rsrc(stage_w<C>(P), (unsigned)(C::KTOT * C::LDW * 2));
    rsrc_t srdA = mk_rsrc(Ain, (unsigned)(S_ * C::KTOT * 2));
    rsrc_t srdC = mk_rsrc(Cout, (unsigned)(S_ * C::LDW * 2));

    const int wm = w % C::WM, wn = w / C::WM;
    const int fr = l & 15, fq = l >> 4;
    const int q = (l & 15) >> 2, p = l & 3;
    const int bh = (C::BN >= 128) ? (q | ((fq & 1) << 2)) : (((q >> 1) & 1) | ((fq & 1) << 1));
    const int a_swz = (fr >> 1) & 7;
    const int erow = r0 + wm * C::MT * 16 + fr;
    const int ecol = n0 + wn * C::NT * 16 + 4 * fq;

    u32x2 res0[C::MT][C::NT], res1[C::MT][C::NT];
    f32x2 st_in[C::MT];
    if constexpr (C::EPI == EP_S2 || C::EPI == EP_S4 || C::EPI == EP_S6) {
        const u16* r0p = (C::EPI == EP_S2) ? xres : (const u16*)(lay + OFF_X2);
        rsrc_t srdR0 = mk_rsrc(r0p, (unsigned)(S_ * D_ * 2));
#pragma unroll
        for (int mt = 0; mt < C::MT; ++mt)
#pragma unroll
            for (int nt = 0; nt < C::NT; ++nt)
                res0[mt][nt] = __builtin_bit_cast(u32x2, __builtin_amdgcn_raw_buffer_load_b64(srdR0, (unsigned)(((erow + 16 * mt) * D_ + ecol + 16 * nt) * 2), 0, SC1AUX));
        if constexpr (C::EPI == EP_S6) {
            rsrc_t srdR1 = mk_rsrc(lay + OFF_X1, (unsigned)(S_ * D_ * 2));
#pragma unroll
            for (int mt = 0; mt < C::MT; ++mt)
#pragma unroll
                for (int nt = 0; nt < C::NT; ++nt)
                    res1[mt][nt] = __builtin_bit_cast(u32x2, __builtin_amdgcn_raw_buffer_load_b64(srdR1, (unsigned)(((erow + 16 * mt) * D_ + ecol + 16 * nt) * 2), 0, SC1AUX));
        }
        if constexpr (C::EPI == EP_S4) {
            rsrc_t srdS = mk_rsrc(lay + OFF_ST, (unsigned)(S_ * 8));
#pragma unroll
            for (int mt = 0; mt < C::MT; ++mt) {
                st_in[mt] = __builtin_bit_cast(f32x2, __builtin_amdgcn_raw_buffer_load_b64(srdS, (unsigned)((erow + 16 * mt) * 8), 0, SC1AUX));
            }
        }
    }

    f32x4 cs_in[C::NT];
    if constexpr (C::LN) {
        const float* cst = (const float*)(P.ws + WS_CS) + ((C::EPI == EP_S3) ? CS_W1 : CS_W3);
        rsrc_t srdCS = mk_rsrc(cst, (unsigned)(C::LDW * 4));
#pragma unroll
        for (int nt = 0; nt < C::NT; ++nt)
            cs_in[nt] = __builtin_bit_cast(f32x4, __builtin_amdgcn_raw_buffer_load_b128(srdCS, (unsigned)((ecol + 16 * nt) * 4), 0, 0));
    }
    unsigned bvoff[C::BPW];
    g_bvoff<C>(bvoff, n0, w, l);
    const int arow = 8 * w + (l >> 3);
    const int alu = (l & 7) ^ ((arow >> 1) & 7);
    const unsigned avoff = (unsigned)(((r0 + arow) * C::KTOT + alu * 8) * 2);
    auto issueA = [&](auto KC) {
        constexpr int kc = decltype(KC)::value;
        const unsigned dst = lds0 + (unsigned)((kc % C::D) * C::SLOT + w * 1024);
        if constexpr (C::AMODE == AM_SHIFT) {
            const int k0 = kc * 64 + alu * 8;
            const int grp = k0 / 112;
            const int sh = (grp == 0) ? -1 : (grp == 1) ? 1 : (grp == 2) ? -2 : 2;
            const int srow = r0 + arow + sh;
            const unsigned vo = (srow < 0 || srow >= S_) ? 0xFFFFFFF0u : (unsigned)((srow * C::KTOT + alu * 8) * 2);
            dma16<SC1DMA>(srdA, vo, (unsigned)(kc * 128), dst);
        } else {
            dma16<SC1DMA>(srdA, avoff, (unsigned)(kc * 128), dst);
        }
    };
    auto issueB = [&](auto KC) { g_issueB<C>(srdW, bvoff, decltype(KC)::value, lds0, w); };

    sfor<0, (C::D < C::NCHUNK ? C::D : C::NCHUNK)>([&](auto KC) { issueA(KC); });

    f32x4 acc[C::MT][C::NT];
#pragma unroll
    for (int mt = 0; mt < C::MT; ++mt)
#pragma unroll
        for (int nt = 0; nt < C::NT; ++nt) acc[mt][nt] = (f32x4){0.f, 0.f, 0.f, 0.f};
    float ssum[C::MT], ssq[C::MT];
#pragma unroll
    for (int mt = 0; mt < C::MT; ++mt) { ssum[mt] = 0.f; ssq[mt] = 0.f; }

    const int a_off0 = (wm * C::MT * 16 + fr) * 128 + ((fq ^ a_swz) * 16);
    const int a_off1 = (wm * C::MT * 16 + fr) * 128 + (((4 + fq) ^ a_swz) * 16);
    int b_off[C::NT];
#pragma unroll
    for (int nt = 0; nt < C::NT; ++nt) b_off[nt] = (8 * fq + q) * (2 * C::BN) + (((wn * C::NT + nt) ^ bh) * 32) + p * 8;

    ring_loop<C::NCHUNK, C::D, C::APW, C::BPW>(issueA, issueB, [&](auto KC) {
        constexpr int kc = decltype(KC)::value;
        const char* cA = smem + (kc % C::D) * C::SLOT;
        const char* cB = cA + C::A_BYTES;
        bf16x8 xa[2][C::MT], wb[2][C::NT];
#pragma unroll
        for (int s = 0; s < 2; ++s) {
#pragma unroll
            for (int mt = 0; mt < C::MT; ++mt) xa[s][mt] = *(const bf16x8*)(cA + (s ? a_off1 : a_off0) + mt * 2048);
#pragma unroll
            for (int nt = 0; nt < C::NT; ++nt)
                wb[s][nt] = ld_tr(cB + b_off[nt] + s * (32 * 2 * C::BN), cB + b_off[nt] + s * (32 * 2 * C::BN) + 4 * 2 * C::BN);
        }
#pragma unroll
        for (int s = 0; s < 2; ++s) {
#pragma unroll
            for (int mt = 0; mt < C::MT; ++mt)
#pragma unroll
                for (int nt = 0; nt < C::NT; ++nt)
                    acc[mt][nt] = __builtin_amdgcn_mfma_f32_16x16x32_bf16(wb[s][nt], xa[s][mt], acc[mt][nt], 0, 0, 0);
            if constexpr (C::LN) {
                if (wn == ((2 * kc + s) % C::WN)) {
#pragma unroll
                    for (int mt = 0; mt < C::MT; ++mt) stat_acc(xa[s][mt], ssum[mt], ssq[mt]);
                }
            }
        }
    });

    float mean[C::MT], rstd[C::MT];
    if constexpr (C::LN) {
        f32x2* sst = (f32x2*)(smem + ((C::EPI == EP_S5) ? 3 * C::SLOT : C::D * C::SLOT));
#pragma unroll
        for (int mt = 0; mt < C::MT; ++mt) {
            float s = grp_reduce(ssum[mt]), ss = grp_reduce(ssq[mt]);
            if (fq == 0) { f32x2 t; t[0] = s; t[1] = ss; sst[wn * C::BM + wm * C::MT * 16 + mt * 16 + fr] = t; }
        }
        wg_barrier();
#pragma unroll
        for (int mt = 0; mt < C::MT; ++mt) {
            float s = 0.f, ss = 0.f;
#pragma unroll
            for (int i = 0; i < C::WN; ++i) { f32x2 t = sst[i * C::BM + wm * C::MT * 16 + mt * 16 + fr]; s += t[0]; ss += t[1]; }
            float m = s * (1.f / D_);
            float v = fmaxf(ss * (1.f / D_) - m * m, 0.f);
            mean[mt] = m;
            if constexpr (C::EPI == EP_S3) {
                rstd[mt] = __builtin_amdgcn_rsqf(v * (1.f + LN_EPS) + LN_EPS * LN_EPS);
                if (tn == 0 && wn == 0 && fq == 0) {
                    rsrc_t srdS = mk_rsrc(lay + OFF_ST, (unsigned)(S_ * 8));
                    f32x2 sv; sv[0] = m; sv[1] = __builtin_amdgcn_rsqf(v + LN_EPS);
                    __builtin_amdgcn_raw_buffer_store_b64(__builtin_bit_cast(u32x2, sv), srdS, (unsigned)((erow + 16 * mt) * 8), 0, SC1AUX);
                }
            } else {
                rstd[mt] = __builtin_amdgcn_rsqf(v + LN_EPS);
            }
        }
    }
    u32x2 ov[C::MT][C::NT];
#pragma unroll
    for (int nt = 0; nt < C::NT; ++nt) {
#pragma unroll
        for (int mt = 0; mt < C::MT; ++mt) {
            float v[4];
#pragma unroll
            for (int r = 0; r < 4; ++r) v[r] = acc[mt][nt][r];
            u32x2 o;
            if constexpr (C::EPI == EP_S3 || C::EPI == EP_S5) {
                const f32x4 cs = cs_in[nt];
                f32x2 a0 = {acc[mt][nt][0], acc[mt][nt][1]}, a1 = {acc[mt][nt][2], acc[mt][nt][3]};
                f32x2 c0 = {cs[0], cs[1]}, c1 = {cs[2], cs[3]};
                o[0] = ln_gelu_pk(a0, c0, mean[mt], rstd[mt]);
                o[1] = ln_gelu_pk(a1, c1, mean[mt], rstd[mt]);
            } else if constexpr (C::EPI == EP_S2) {
                u32x2 x = res0[mt][nt];
                o[0] = pack2(lo_f(x[0]) + rbf(v[0]), hi_f(x[0]) + rbf(v[1]));
                o[1] = pack2(lo_f(x[1]) + rbf(v[2]), hi_f(x[1]) + rbf(v[3]));
            } else if constexpr (C::EPI == EP_S4) {
                u32x2 x = res0[mt][nt];
                const float m = st_in[mt][0], rs = st_in[mt][1];
                o[0] = pack2(rbf((lo_f(x[0]) - m) * rs) + rbf(v[0]), rbf((hi_f(x[0]) - m) * rs) + rbf(v[1]));
                o[1] = pack2(rbf((lo_f(x[1]) - m) * rs) + rbf(v[2]), rbf((hi_f(x[1]) - m) * rs) + rbf(v[3]));
            } else {
                u32x2 x = res0[mt][nt], y = res1[mt][nt];
                o[0] = pack2(lo_f(x[0]) + rbf(lo_f(y[0]) + rbf(v[0])), hi_f(x[0]) + rbf(hi_f(y[0]) + rbf(v[1])));
                o[1] = pack2(lo_f(x[1]) + rbf(lo_f(y[1]) + rbf(v[2])), hi_f(x[1]) + rbf(hi_f(y[1]) + rbf(v[3])));
            }
            ov[mt][nt] = o;
        }
    }
#pragma unroll
    for (int mt = 0; mt < C::MT; ++mt)
#pragma unroll
        for (int nt = 0; nt < C::NT; nt += 2) {
            auto r0 = __builtin_amdgcn_permlane16_swap(ov[mt][nt][0], ov[mt][nt + 1][0], false, false);
            auto r1 = __builtin_amdgcn_permlane16_swap(ov[mt][nt][1], ov[mt][nt + 1][1], false, false);
            u32x4 o4; o4[0] = r0[0]; o4[1] = r1[0]; o4[2] = r0[1]; o4[3] = r1[1];
            const int col = n0 + wn * C::NT * 16 + 16 * (nt + (fq & 1)) + 8 * (fq >> 1);
            __builtin_amdgcn_raw_buffer_store_b128(o4, srdC, (unsigned)(((erow + 16 * mt) * C::LDW + col) * 2), 0, SC1AUX);
        }
}

struct S1C {
    static constexpr int BM = 80, BN = 192, NCHUNK = D_ / 64, D = 4, BPW = 3;
    static constexpr int A_BYTES = BM * 128, B_BYTES = 64 * BN * 2, SLOT = A_BYTES + B_BYTES;
    static constexpr int QS = 392;
    static constexpr int STG_BYTES = BM * QS;
    static constexpr int LDS_TOP = 163840;
    static constexpr int STG_OFF = LDS_TOP - STG_BYTES;
    static constexpr int STAT_OFF = STG_OFF - 4 * 80 * 8;
};

__device__ __forceinline__ void s1_bvoff(unsigned (&bvoff)[3], int hp, int w, int l) {
#pragma unroll
    for (int i = 0; i < 3; ++i) {
        int u = 64 * (w * 3 + i) + l;
        int k = u / 24, pu = u % 24;
        int h = ((k >> 1) & 1) | (((k >> 3) & 1) << 1);
        int lu32 = (pu >> 1) ^ h;
        int c = lu32 * 16 + (pu & 1) * 8;
        int gcol = (c >> 6) * D_ + hp * 56 + (c & 63);
        bvoff[i] = (unsigned)((k * (3 * D_) + gcol) * 2);
    }
}
__device__ __forceinline__ void s1_issueB(rsrc_t srdW, const unsigned (&bvoff)[3], int kc, unsigned lds0, int w) {
#pragma unroll
    for (int i = 0; i < 3; ++i)
        dma16<0>(srdW, bvoff[i], (unsigned)(kc * 64 * 3 * D_ * 2), lds0 + (unsigned)((kc % S1C::D) * S1C::SLOT + S1C::A_BYTES + (w * 3 + i) * 1024));
}
__device__ __forceinline__ void s1_prefetch(const Params& P, int tile, unsigned lds0) {
    const int w = __builtin_amdgcn_readfirstlane(threadIdx.x >> 6), l = threadIdx.x & 63;
    rsrc_t srdW = mk_rsrc(P.wqkv, (unsigned)(D_ * 3 * D_ * 2));
    unsigned bvoff[3];
    s1_bvoff(bvoff, tile & 7, w, l);
#pragma unroll
    for (int j = 0; j < S1C::D; ++j) s1_issueB(srdW, bvoff, j, lds0, w);
}

template <int APW, int MT>
__device__ __forceinline__ void s1_gemm(const Params& P, const u16* xin, int r0, int hp, char* smem, int w, int l) {
    const unsigned lds0 = (unsigned)(size_t)smem;
    rsrc_t srdW = mk_rsrc(P.wqkv, (unsigned)(D_ * 3 * D_ * 2));
    rsrc_t srdA = mk_rsrc(xin, (unsigned)(S_ * D_ * 2));
    const int ng = w & 3, mg = w >> 2;
    const int mt0 = mg * 3;
    const int fr = l & 15, fq = l >> 4, q = (l & 15) >> 2, p = l & 3;
    const int bh = ((q >> 1) & 1) | ((fq & 1) << 1);
    const int a_swz = (fr >> 1) & 7;
    unsigned bvoff[3];
    s1_bvoff(bvoff, hp, w, l);
    unsigned avoff[APW];
#pragma unroll
    for (int i = 0; i < APW; ++i) {
        const int j = (i == 0) ? w : 8 + w;
        const int arow = 8 * j + (l >> 3);
        const int alu = (l & 7) ^ ((arow >> 1) & 7);
        const int grow = r0 - 16 + arow;
        avoff[i] = (grow < 0) ? 0xFFFFFFF0u : (unsigned)((grow * D_ + alu * 8) * 2);
    }
    auto issueA = [&](auto KC) {
        constexpr int kc = decltype(KC)::value;
#pragma unroll
        for (int i = 0; i < APW; ++i) {
            const int j = (i == 0) ? w : 8 + w;
            dma16<SC1DMA>(srdA, avoff[i], (unsigned)(kc * 128), lds0 + (unsigned)((kc % S1C::D) * S1C::SLOT + j * 1024));
        }
    };
    auto issueB = [&](auto KC) { s1_issueB(srdW, bvoff, decltype(KC)::value, lds0, w); };
    sfor<0, S1C::D>([&](auto KC) { issueA(KC); });

    f32x4 acc[MT][3];
#pragma unroll
    for (int mt = 0; mt < MT; ++mt)
#pragma unroll
        for (int nt = 0; nt < 3; ++nt) acc[mt][nt] = (f32x4){0.f, 0.f, 0.f, 0.f};
    float ssum[MT], ssq[MT];
#pragma unroll
    for (int mt = 0; mt < MT; ++mt) { ssum[mt] = 0.f; ssq[mt] = 0.f; }
    f32x4 cs_in[3];
    {
        rsrc_t srdCS = mk_rsrc(P.ws + WS_CS, (unsigned)(1344 * 4));
#pragma unroll
        for (int nt = 0; nt < 3; ++nt) {
            const int c = (ng * 3 + nt) * 16 + 4 * fq;
            const int gcol = (c >> 6) * D_ + hp * 56 + (c & 63);
            cs_in[nt] = __builtin_bit_cast(f32x4, __builtin_amdgcn_raw_buffer_load_b128(srdCS, (unsigned)(gcol * 4), 0, 0));
        }
    }
    const int a_off0 = (mt0 * 16 + fr) * 128 + ((fq ^ a_swz) * 16);
    const int a_off1 = (mt0 * 16 + fr) * 128 + (((4 + fq) ^ a_swz) * 16);
    int b_off[3];
#pragma unroll
    for (int nt = 0; nt < 3; ++nt) b_off[nt] = (8 * fq + q) * (2 * S1C::BN) + (((ng * 3 + nt) ^ bh) * 32) + p * 8;

    ring_loop<S1C::NCHUNK, S1C::D, APW, 3>(issueA, issueB, [&](auto KC) {
        constexpr int kc = decltype(KC)::value;
        const char* cA = smem + (kc % S1C::D) * S1C::SLOT;
        const char* cB = cA + S1C::A_BYTES;
        bf16x8 xa[2][MT], wb[2][3];
#pragma unroll
        for (int s = 0; s < 2; ++s) {
#pragma unroll
            for (int mt = 0; mt < MT; ++mt) xa[s][mt] = *(const bf16x8*)(cA + (s ? a_off1 : a_off0) + mt * 2048);
#pragma unroll
            for (int nt = 0; nt < 3; ++nt)
                wb[s][nt] = ld_tr(cB + b_off[nt] + s * (32 * 2 * S1C::BN), cB + b_off[nt] + s * (32 * 2 * S1C::BN) + 4 * 2 * S1C::BN);
        }
#pragma unroll
        for (int s = 0; s < 2; ++s) {
#pragma unroll
            for (int mt = 0; mt < MT; ++mt)
#pragma unroll
                for (int nt = 0; nt < 3; ++nt)
                    acc[mt][nt] = __builtin_amdgcn_mfma_f32_16x16x32_bf16(wb[s][nt], xa[s][mt], acc[mt][nt], 0, 0, 0);
            if (ng == ((2 * kc + s) & 3)) {
#pragma unroll
                for (int mt = 0; mt < MT; ++mt) stat_acc(xa[s][mt], ssum[mt], ssq[mt]);
            }
        }
    });
    char* stg = smem + S1C::STG_OFF;
    f32x2* sst = (f32x2*)(smem + S1C::STAT_OFF);
#pragma unroll
    for (int mt = 0; mt < MT; ++mt) {
        float s = grp_reduce(ssum[mt]), ss = grp_reduce(ssq[mt]);
        if (fq == 0) { f32x2 t; t[0] = s; t[1] = ss; sst[ng * 80 + (mt0 + mt) * 16 + fr] = t; }
    }
    wg_barrier();
    float mean[MT], rstd[MT];
#pragma unroll
    for (int mt = 0; mt < MT; ++mt) {
        float s = 0.f, ss = 0.f;
#pragma unroll
        for (int i = 0; i < 4; ++i) { f32x2 t = sst[i * 80 + (mt0 + mt) * 16 + fr]; s += t[0]; ss += t[1]; }
        float m = s * (1.f / D_);
        float v = fmaxf(ss * (1.f / D_) - m * m, 0.f);
        mean[mt] = m; rstd[mt] = __builtin_amdgcn_rsqf(v + LN_EPS);
    }
#pragma unroll
    for (int nt = 0; nt < 3; ++nt) {
        const f32x4 cs = cs_in[nt];
#pragma unroll
        for (int mt = 0; mt < MT; ++mt) {
            u32x2 o;
            o[0] = pack2(rstd[mt] * (acc[mt][nt][0] - mean[mt] * cs[0]), rstd[mt] * (acc[mt][nt][1] - mean[mt] * cs[1]));
            o[1] = pack2(rstd[mt] * (acc[mt][nt][2] - mean[mt] * cs[2]), rstd[mt] * (acc[mt][nt][3] - mean[mt] * cs[3]));
            *(u32x2*)(stg + ((mt0 + mt) * 16 + fr) * S1C::QS + ((ng * 3 + nt) * 16 + 4 * fq) * 2) = o;
        }
    }
}

__device__ __forceinline__ void s1_attention(int r0, int hp, char* smem, u16* oout, int tid) {
    const char* stg = smem + S1C::STG_OFF;
    const int row = tid >> 3, hh = (tid >> 2) & 1, kh = (tid >> 1) & 1, half = tid & 1;
    const int cb = (hh * 28 + half * 16) * 2;
    u32x2 qv[4];
    {
        const char* qp = stg + (16 + row) * S1C::QS + cb;
#pragma unroll
        for (int i = 0; i < 4; ++i) qv[i] = *(const u32x2*)(qp + i * 8);
        if (half) { qv[3][0] = 0u; qv[3][1] = 0u; }
    }
    float lg4[4];
#pragma unroll
    for (int j = 0; j < 4; ++j) {
        const int wd = kh * 4 + j;
        const char* kp = stg + (16 + row - 2 * wd) * S1C::QS + 128 + cb;
        u32x2 kv[4];
#pragma unroll
        for (int i = 0; i < 4; ++i) kv[i] = *(const u32x2*)(kp + i * 8);
        float a0 = 0.f, a1 = 0.f;
#pragma unroll
        for (int i = 0; i < 4; ++i) { a0 = dot2(qv[i][0], kv[i][0], a0); a1 = dot2(qv[i][1], kv[i][1], a1); }
        lg4[j] = a0 + a1;
    }
    float lg[8];
#pragma unroll
    for (int j = 0; j < 4; ++j) {
        float a = lg4[j] + __shfl_xor(lg4[j], 1);
        const int wd = kh * 4 + j;
        float v = rbf(rbf(a) * 0.18898223650461362f);
        v = (r0 + row - 2 * wd < 0) ? -1e9f : v;
        float o = __shfl_xor(v, 2);
        lg[j] = kh ? o : v;
        lg[4 + j] = kh ? v : o;
    }
    float mx = lg[0];
#pragma unroll
    for (int wd = 1; wd < 8; ++wd) mx = fmaxf(mx, lg[wd]);
    float e[8], sum = 0.f;
#pragma unroll
    for (int wd = 0; wd < 8; ++wd) { e[wd] = __expf(lg[wd] - mx); sum += e[wd]; }
    const float inv = __builtin_amdgcn_rcpf(sum);
    float o[8];
#pragma unroll
    for (int i = 0; i < 8; ++i) o[i] = 0.f;
    const int vb = 256 + cb + kh * 16;
#pragma unroll
    for (int wp = 0; wp < 4; ++wp) {
        const unsigned pp = pack2(e[2 * wp] * inv, e[2 * wp + 1] * inv);
        const char* v0 = stg + (16 + row - 4 * wp) * S1C::QS + vb;
        const char* v1 = stg + (16 + row - 4 * wp - 2) * S1C::QS + vb;
#pragma unroll
        for (int i = 0; i < 2; ++i) {
            u32x2 a = *(const u32x2*)(v0 + i * 8), b = *(const u32x2*)(v1 + i * 8);
#pragma unroll
            for (int j = 0; j < 2; ++j) {
                unsigned lo = (a[j] & 0xffffu) | (b[j] << 16);
                unsigned hi = (a[j] >> 16) | (b[j] & 0xffff0000u);
                o[4 * i + 2 * j] = dot2(lo, pp, o[4 * i + 2 * j]);
                o[4 * i + 2 * j + 1] = dot2(hi, pp, o[4 * i + 2 * j + 1]);
            }
        }
    }
    rsrc_t srdO = mk_rsrc(oout, (unsigned)(S_ * D_ * 2));
    const unsigned ob = (unsigned)(((r0 + row) * D_ + hp * 56 + hh * 28 + half * 16 + kh * 8) * 2);
    {
        u32x2 ov; ov[0] = pack2(o[0], o[1]); ov[1] = pack2(o[2], o[3]);
        __builtin_amdgcn_raw_buffer_store_b64(ov, srdO, ob, 0, SC1AUX);
    }
    if (!(half && kh)) {
        u32x2 ov; ov[0] = pack2(o[4], o[5]); ov[1] = pack2(o[6], o[7]);
        __builtin_amdgcn_raw_buffer_store_b64(ov, srdO, ob + 8, 0, SC1AUX);
    }
}

__device__ __forceinline__ void s1_main(const Params& P, int layer, int tile, char* smem) {
    const int tid = threadIdx.x, w = __builtin_amdgcn_readfirstlane(tid >> 6), l = tid & 63;
    const int r0 = (tile >> 3) * 64, hp = tile & 7;
    const u16* xin = (layer == 0) ? P.x : (const u16*)(P.ws + WS_X3);
    u16* oout = (u16*)(P.ws + WS_L0 + (size_t)layer * LAYER_BYTES + OFF_O);
    if (w < 2) s1_gemm<2, 3>(P, xin, r0, hp, smem, w, l);
    else if (w < 4) s1_gemm<1, 3>(P, xin, r0, hp, smem, w, l);
    else s1_gemm<1, 2>(P, xin, r0, hp, smem, w, l);
    wg_barrier();
    s1_attention(r0, hp, smem, oout, tid);
}

__device__ __forceinline__ int xcd_tile(int b, int ntn) {
    const int xcd = b & 7, idx = b >> 3;
    const int panel = xcd * 4 + idx / ntn, tn = idx % ntn;
    return panel * ntn + tn;
}

__global__ __launch_bounds__(512) void k_prep(Params P) {
    __shared__ float red[64][65];
    const int b = blockIdx.x, t = threadIdx.x;
    {
        const int gid = b * 512 + t;
        if (gid < 16 * 32) ((unsigned*)(P.ws + WS_CNT))[gid * 64] = 0u;
    }
    const u16* W; int ldw, c0, o0;
    if (b < 21) { W = P.wqkv; ldw = 3 * D_; c0 = b * 64; o0 = CS_QKV + c0; }
    else if (b < 28) { W = P.w1; ldw = D_; c0 = (b - 21) * 64; o0 = CS_W1 + c0; }
    else { W = P.w3; ldw = F_; c0 = (b - 28) * 64; o0 = CS_W3 + c0; }
    const int cc = (t & 7) * 8, rg = t >> 3;
    u32x4 v[7];
#pragma unroll
    for (int i = 0; i < 7; ++i) v[i] = *(const u32x4*)(W + (size_t)(rg + 64 * i) * ldw + c0 + cc);
    float a[8];
#pragma unroll
    for (int j = 0; j < 8; ++j) a[j] = 0.f;
#pragma unroll
    for (int i = 0; i < 7; ++i)
#pragma unroll
        for (int j = 0; j < 4; ++j) { a[2 * j] += lo_f(v[i][j]); a[2 * j + 1] += hi_f(v[i][j]); }
#pragma unroll
    for (int j = 0; j < 8; ++j) red[rg][cc + j] = a[j];
    __syncthreads();
    if (t < 64) {
        float s = 0.f;
        for (int i = 0; i < 64; ++i) s += red[i][t];
        ((float*)(P.ws + WS_CS))[o0 + t] = s;
    }
}

__device__ __forceinline__ unsigned* cnt_ptr(const Params& P, int sidx, int panel) {
    return (unsigned*)(P.ws + WS_CNT) + (sidx * 32 + panel) * 64;
}
__device__ __forceinline__ void wait_cnt(const Params& P, int sidx, int p0, int p1, unsigned target) {
    if (threadIdx.x == 0) {
        for (int p = p0; p <= p1; ++p) {
            if (p < 0 || p >= 32) continue;
            unsigned* c = cnt_ptr(P, sidx, p);
            unsigned spins = 0;
            while (__hip_atomic_load(c, __ATOMIC_RELAXED, __HIP_MEMORY_SCOPE_AGENT) < target) {
                __builtin_amdgcn_s_sleep(1);
                if (++spins > (1u << 24)) break;
            }
        }
    }
    wg_barrier();
}
__device__ __forceinline__ void signal_cnt(const Params& P, int sidx, int panel) {
    wg_barrier();
    if (threadIdx.x == 0) __hip_atomic_fetch_add(cnt_ptr(P, sidx, panel), 1u, __ATOMIC_RELAXED, __HIP_MEMORY_SCOPE_AGENT);
}

template <class C> __device__ __forceinline__ void g_stage(const Params& P, int layer, int b, char* smem, bool active,
                                                            int dep_s, int dp0, int dp1, unsigned dep_t) {
    if (!active) return;
    const int tile = xcd_tile(b, C::NTN);
    wait_cnt(P, dep_s, (tile / C::NTN) + dp0, (tile / C::NTN) + dp1, dep_t);
    gemm_main<C>(P, layer, tile, smem);
}

__global__ __launch_bounds__(NT_, 2) void k_persist(Params P) {
    __shared__ __attribute__((aligned(16))) char smem[163840];
    const unsigned lds0 = (unsigned)(size_t)smem;
    const int b = blockIdx.x;
    const bool act = (b >> 3) < 28;
    const int t1 = xcd_tile(b, 8);
    s1_prefetch(P, t1, lds0);
    for (int layer = 0; layer < 2; ++layer) {
        const int sb = layer * 6;
        if (layer == 1) wait_cnt(P, 5, (t1 >> 3) - 1, (t1 >> 3), 7u);
        s1_main(P, layer, t1, smem);
        wg_barrier();
        if (act) gemm_prefetch<GCfg<EP_S2>>(P, xcd_tile(b, 7), lds0);
        if (act) wait_vm<GCfg<EP_S2>::D * GCfg<EP_S2>::BPW>(); else wait_vm<0>();
        signal_cnt(P, sb + 0, t1 >> 3);
        if (act) {
            g_stage<GCfg<EP_S2>>(P, layer, b, smem, true, sb + 0, 0, 0, 8u);
            wg_barrier();
            gemm_prefetch<GCfg<EP_S3>>(P, xcd_tile(b, 7), lds0);
            wait_vm<GCfg<EP_S3>::D * GCfg<EP_S3>::BPW>();
            signal_cnt(P, sb + 1, xcd_tile(b, 7) / 7);
            g_stage<GCfg<EP_S3>>(P, layer, b, smem, true, sb + 1, 0, 0, 7u);
            wg_barrier();
            gemm_prefetch<GCfg<EP_S4>>(P, xcd_tile(b, 7), lds0);
            wait_vm<GCfg<EP_S4>::D * GCfg<EP_S4>::BPW>();
            signal_cnt(P, sb + 2, xcd_tile(b, 7) / 7);
            g_stage<GCfg<EP_S4>>(P, layer, b, smem, true, sb + 2, -1, 1, 7u);
            wg_barrier();
            gemm_prefetch<GCfg<EP_S5>>(P, xcd_tile(b, 7), lds0);
            wait_vm<GCfg<EP_S5>::D * GCfg<EP_S5>::BPW>();
            signal_cnt(P, sb + 3, xcd_tile(b, 7) / 7);
            g_stage<GCfg<EP_S5>>(P, layer, b, smem, true, sb + 3, 0, 0, 7u);
            wg_barrier();
            gemm_prefetch<GCfg<EP_S6>>(P, xcd_tile(b, 7), lds0);
            wait_vm<GCfg<EP_S6>::D * GCfg<EP_S6>::BPW>();
            signal_cnt(P, sb + 4, xcd_tile(b, 7) / 7);
            g_stage<GCfg<EP_S6>>(P, layer, b, smem, true, sb + 4, 0, 0, 7u);
            wg_barrier();
            if (layer == 0) { s1_prefetch(P, t1, lds0); wait_vm<S1C::D * S1C::BPW>(); } else wait_vm<0>();
            signal_cnt(P, sb + 5, xcd_tile(b, 7) / 7);
        } else if (layer == 0) {
            s1_prefetch(P, t1, lds0);
        }
    }
}

}

extern "C" void kernel_launch(void* const* d_in, const int* in_sizes, int n_in,
                              void* d_out, int out_size, void* d_ws, size_t ws_size,
                              hipStream_t stream) {
    Params P;
    P.x = (const u16*)d_in[0]; P.wqkv = (const u16*)d_in[1]; P.wo = (const u16*)d_in[2];
    P.w1 = (const u16*)d_in[4]; P.w2 = (const u16*)d_in[5]; P.w3 = (const u16*)d_in[6]; P.w4 = (const u16*)d_in[7];
    P.out = (u16*)d_out; P.ws = (char*)d_ws;
    k_prep<<<56, 512, 0, stream>>>(P);
    k_persist<<<256, NT_, 0, stream>>>(P);

}
```
